```python
import torch

def _softmax(x, axis=-1):
    return torch.softmax(x, dim=axis)
def _gelu(x):
    return torch.nn.functional.gelu(x, approximate="tanh")
def _silu(x):
    return torch.nn.functional.silu(x)
def _sigmoid(x):
    return torch.sigmoid(x)
def _layernorm(x, axis=-1, eps=1e-5):
    m = x.mean(dim=axis, keepdim=True)
    v = x.var(dim=axis, keepdim=True, unbiased=False)
    return (x - m) / torch.sqrt(v + eps)
def _scatter_add(tgt, idx, src, axis=0):
    return tgt.clone().index_add_(axis, idx, src)
def _segment_sum(src, seg, n):
    out = torch.zeros((n,) + src.shape[1:], dtype=src.dtype)
    return out.index_add_(0, seg, src)
def _pad(x, pw):
    flat = tuple(v for pair in reversed(pw) for v in pair)
    return torch.nn.functional.pad(x, flat)

kernel_name = 'gated_mix__grouped_kv_mqa__mha_block__prompt'

def setup_inputs():

    _w = (lambda a: (lambda t: t.to(torch.bfloat16) if t.is_floating_point() else t)(torch.as_tensor(a)))
    def _A_setup():
        B, S, D, H = 1, 2048, 640, 32
        Dh = D // H
        return {
            "x": _w((lambda *s: torch.randn(*s, dtype=torch.bfloat16) * 0.1)(B, S, D)),
            "wq": _w((lambda *s: torch.randn(*s, dtype=torch.bfloat16) * 0.4)(D, H * Dh)),
            "wk": _w((lambda *s: torch.randn(*s, dtype=torch.bfloat16) * 0.4)(D, Dh)),
            "wv": _w((lambda *s: torch.randn(*s, dtype=torch.bfloat16) * 0.4)(D, Dh)),
            "wo": _w((lambda *s: torch.randn(*s, dtype=torch.bfloat16) * 0.4)(H * Dh, D)),
        }
    _A_in = _A_setup()

    def _B_setup():
        B, S, D, H = 1, 2048, 640, 32
        return {
            "x": _w((lambda *s: torch.randn(*s, dtype=torch.bfloat16) * 0.1)(B, S, D)),
            "wqkv": _w((lambda *s: torch.randn(*s, dtype=torch.bfloat16) * 0.4)(D, 3 * D)),
            "wo": _w((lambda *s: torch.randn(*s, dtype=torch.bfloat16) * 0.2)(D, D)),
        }
    _B_in = _B_setup()

    return {
        "A_x": _A_in["x"],
        "A_wq": _A_in["wq"],
        "A_wk": _A_in["wk"],
        "A_wv": _A_in["wv"],
        "A_wo": _A_in["wo"],
        "B_x": _B_in["x"],
        "B_wqkv": _B_in["wqkv"],
        "B_wo": _B_in["wo"],
        "gate_w": _w((lambda *s: torch.randn(*s, dtype=torch.bfloat16) * 0.1)(640) * 16.0),
    }

def reference(A_x, A_wq, A_wk, A_wv, A_wo, B_x, B_wqkv, B_wo, gate_w):
    def _A_ref(x, wq, wk, wv, wo):
        B, S, D = x.shape
        H = 32
        Dh = D // H
        q = (lambda x, *d: x.permute(*d))((x @ wq).reshape(B, S, H, Dh), 0, 2, 1, 3)
        k = x @ wk
        v = x @ wv
        scores = torch.einsum("bhqd,bkd->bhqk", q, k) * (Dh ** -0.5)
        p = _softmax(scores, axis=-1)
        o = torch.einsum("bhqk,bkd->bhqd", p, v)
        return (lambda x, *d: x.permute(*d))(o, 0, 2, 1, 3).reshape(B, S, H * Dh) @ wo

    def _B_ref(x, wqkv, wo):
        B, S, D = x.shape
        H = 32
        Dh = D // H
        qkv = (lambda x, *d: x.permute(*d))((x @ wqkv).reshape(B, S, 3, H, Dh), 2, 0, 3, 1, 4)
        q, k, v = qkv[0], qkv[1], qkv[2]
        scores = torch.einsum("bhqd,bhkd->bhqk", q, k) * (Dh ** -0.5)
        p = _softmax(scores, axis=-1)
        o = torch.einsum("bhqk,bhkd->bhqd", p, v)
        return (lambda x, *d: x.permute(*d))(o, 0, 2, 1, 3).reshape(B, S, D) @ wo

    x = A_x
    g = _sigmoid((x * gate_w).sum(-1, keepdims=True))
    a = _A_ref(x, A_wq, A_wk, A_wv, A_wo)
    b = _B_ref(x, B_wqkv, B_wo)
    return g * a + (1 - g) * b
```

```cpp
#include <hip/hip_runtime.h>
#include <hip/hip_bf16.h>
#include <stdint.h>

typedef short bf16x8 __attribute__((ext_vector_type(8)));
typedef short bf16x4 __attribute__((ext_vector_type(4)));
typedef __bf16 bfv8 __attribute__((ext_vector_type(8)));
typedef __bf16 bfv16 __attribute__((ext_vector_type(16)));
typedef float f32x16 __attribute__((ext_vector_type(16)));
typedef float f32x8 __attribute__((ext_vector_type(8)));
typedef float f32x4 __attribute__((ext_vector_type(4)));
typedef unsigned u32x4 __attribute__((ext_vector_type(4)));
typedef unsigned u32x2 __attribute__((ext_vector_type(2)));
typedef unsigned short u16;
#define LAS __attribute__((address_space(3)))
typedef LAS const char* lds_cptr;

constexpr int SEQ = 2048, DM = 640;
constexpr int QT_B = 1280;
constexpr int VROWS = 22;
constexpr int VBLK = VROWS * 16;
constexpr int KV_B = 1280 + 4 * VBLK;
constexpr size_t WS_Q = 0;
constexpr size_t WS_KV = WS_Q + (size_t)64 * 64 * QT_B;
constexpr size_t WS_O = WS_KV + (size_t)33 * 64 * KV_B + 16384;
constexpr size_t WS_WO = WS_O + (size_t)128 * 40 * 1024;
constexpr size_t WS_G = WS_WO + (size_t)40 * 40 * 1024;
constexpr float QSCALE = 0.22360679774997896f * 1.4426950408889634f;

#define MFMA16(a, b, c) __builtin_amdgcn_mfma_f32_32x32x16_bf16(__builtin_bit_cast(bfv8, a), __builtin_bit_cast(bfv8, b), c, 0, 0, 0)
#define MFMA8(a, b, c) __builtin_amdgcn_mfma_f32_32x32x8bf16_1k(a, b, c, 0, 0, 0)
#define SBAR() __builtin_amdgcn_sched_barrier(0)
#define PIN(x) asm volatile("" : "+v"(x))

__device__ __forceinline__ unsigned cvtpk(float lo, float hi) {
    typedef __bf16 bf2 __attribute__((ext_vector_type(2)));
    bf2 v; v[0] = (__bf16)lo; v[1] = (__bf16)hi;
    return __builtin_bit_cast(unsigned, v);
}
__device__ __forceinline__ float bf2f(unsigned v16) { return __uint_as_float(v16 << 16); }
__device__ __forceinline__ float bfr(float x) { return bf2f(cvtpk(x, 0.f) & 0xffffu); }

__device__ __forceinline__ void glds16(const void* sbase, unsigned voff, unsigned lds_dst) {
    unsigned sv;
    asm volatile("s_mov_b32 %0, m0\n\ts_mov_b32 m0, %3\n\ts_nop 0\n\tglobal_load_lds_dwordx4 %1, %2\n\ts_mov_b32 m0, %0"
                 : "=&s"(sv) : "v"(voff), "s"(sbase), "s"(lds_dst) : "memory");
}
__device__ __forceinline__ void glds16_40(const void* sbase, unsigned voff, unsigned lds_dst) {
    unsigned sv;
    asm volatile("s_mov_b32 %0, m0\n\ts_mov_b32 m0, %3\n\ts_mov_b32 exec_hi, 0xff\n\tglobal_load_lds_dwordx4 %1, %2\n\ts_mov_b32 exec_hi, -1\n\ts_mov_b32 m0, %0"
                 : "=&s"(sv) : "v"(voff), "s"(sbase), "s"(lds_dst) : "memory");
}
template <int N> __device__ __forceinline__ void wait_bar() {
    asm volatile("s_waitcnt vmcnt(%0) lgkmcnt(0)\n\ts_barrier" :: "n"(N) : "memory");
}
typedef short v4i16_t __attribute__((ext_vector_type(4)));
__device__ __forceinline__ bf16x4 ldtr(lds_cptr p) { return __builtin_amdgcn_ds_read_tr16_b64_v4i16((LAS v4i16_t*)p); }

constexpr int K1_X = 0, K1_W = 16384, K1_E = 36864, K1_SLOT = 39488, K1_NS = 4;

template <int GRP, bool VOR, bool EXTRA>
__device__ __forceinline__ void k1_body(char* lds, const u16* __restrict__ x, const u16* __restrict__ wsrc, int ldw, const u16* __restrict__ esrc,
                                        int mt, int kind  , int head0, char* __restrict__ ws, int wv, int lane) {
    constexpr int NT = (GRP == 0) ? 3 : (EXTRA ? 3 : 2);
    constexpr int CNT = (GRP == 0) ? 5 : (EXTRA ? 5 : 4);
    const unsigned lds0 = (unsigned)(uintptr_t)lds;
    const int r32 = lane & 31, hh = lane >> 5;
    unsigned voff[5];
    if (GRP == 0) {
#pragma unroll
        for (int i = 0; i < 5; ++i) { const int id = 64 * (wv + 4 * i) + lane; const int row = id / 20, c = id - row * 20; voff[i] = (unsigned)(row * ldw * 2 + 16 * c); }
    } else {
#pragma unroll
        for (int i = 0; i < 4; ++i) { const int row = 8 * (wv + 4 * i) + (lane >> 3); const int c = (lane & 7) ^ ((row >> 1) & 7); voff[i] = (unsigned)((128 * mt + row) * (DM * 2) + 16 * c); }
        voff[4] = (unsigned)(wv * 640 + lane * 16);
    }
    auto issue = [&](int s) {
        const unsigned slot = lds0 + (s % K1_NS) * K1_SLOT;
        if (GRP == 0) {
            const char* b = (const char*)wsrc + (size_t)s * 64 * ldw * 2;
#pragma unroll
            for (int i = 0; i < 5; ++i) glds16(b, voff[i], slot + K1_W + 1024 * (wv + 4 * i));
        } else {
            const char* b = (const char*)x + s * 128;
#pragma unroll
            for (int i = 0; i < 4; ++i) glds16(b, voff[i], slot + K1_X + 1024 * (wv + 4 * i));
            if (EXTRA) glds16_40((const char*)esrc + s * 2560, voff[4], slot + K1_E + wv * 640);
        }
    };
    issue(0); issue(1); issue(2); issue(3);

    f32x16 acc[NT];
#pragma unroll
    for (int t = 0; t < NT; ++t) acc[t] = f32x16{};
    const int sw = (r32 >> 1) & 7;
    const lds_cptr xb = (lds_cptr)lds + K1_X + wv * 4096 + r32 * 128;
    const int q4 = (lane & 15) >> 2, p4 = lane & 3, blk = (lane >> 4) & 1;
    const lds_cptr wb = (lds_cptr)lds + K1_W + (8 * hh + q4) * 320 + blk * 32 + p4 * 8 + GRP * 192;
    const lds_cptr eb = (lds_cptr)lds + K1_E + (8 * hh + q4) * 40 + blk * 32 + p4 * 8;
#pragma unroll
    for (int s = 0; s < 10; ++s) {
        if (s == 0) wait_bar<3 * CNT>(); else if (s <= 7) wait_bar<2 * CNT>(); else if (s == 8) wait_bar<CNT>(); else wait_bar<0>();
        if (s >= 1 && s + 3 < 10) issue(s + 3);
        const int so = (s % K1_NS) * K1_SLOT;
#pragma unroll
        for (int kk = 0; kk < 4; ++kk) {
            const bf16x8 xf = *(LAS const bf16x8*)(xb + so + 16 * ((2 * kk + hh) ^ sw));
#pragma unroll
            for (int t = 0; t < NT; ++t) {
                bf16x4 lo, hi;
                if (GRP == 1 && t == 2) { lo = ldtr(eb + so + kk * 640); hi = ldtr(eb + so + kk * 640 + 160); }
                else { lo = ldtr(wb + so + kk * 5120 + t * 64); hi = ldtr(wb + so + kk * 5120 + 1280 + t * 64); }
                const bf16x8 wf = __builtin_shufflevector(lo, hi, 0, 1, 2, 3, 4, 5, 6, 7);
                acc[t] = VOR ? MFMA16(xf, wf, acc[t]) : MFMA16(wf, xf, acc[t]);
            }
        }
    }
    const int tile = 4 * mt + wv;
    if (!VOR) {
        const float sc = (kind == 0) ? QSCALE : 1.0f;
        const int pos = (kind == 0) ? r32 : ((r32 & 19) | ((r32 & 4) << 1) | ((r32 & 8) >> 1));
        const size_t hstride = (kind == 0) ? (size_t)64 * QT_B : (size_t)64 * KV_B;
        char* base = ws + ((kind == 0) ? WS_Q + (size_t)tile * QT_B : WS_KV + (size_t)tile * KV_B) + (size_t)head0 * hstride;
        u32x2 qd[NT * 4];
#pragma unroll
        for (int t = 0; t < NT; ++t)
#pragma unroll
            for (int g = 0; g < 4; ++g) { qd[t * 4 + g][0] = cvtpk(acc[t][4 * g] * sc, acc[t][4 * g + 1] * sc); qd[t * 4 + g][1] = cvtpk(acc[t][4 * g + 2] * sc, acc[t][4 * g + 3] * sc); }
        constexpr int U0 = GRP * 12;
#pragma unroll
        for (int m = 0; m < 4; ++m) {
            char* be = base + (size_t)(2 * m) * hstride; char* bo = be + hstride;
            const int u = 5 * m - U0;
            if (u >= 0 && u + 1 < ((GRP == 0) ? 12 : 8)) { u32x4 v = {qd[u][0], qd[u][1], qd[u + 1][0], qd[u + 1][1]}; *(u32x4*)(be + hh * 512 + pos * 16) = v; }
            if (u + 2 >= 0 && u + 4 < ((GRP == 0) ? 12 : 8)) {
                const u32x2 a = hh ? qd[u + 2] : qd[u + 3], b = hh ? qd[u + 3] : qd[u + 4];
                u32x4 v = {a[0], a[1], b[0], b[1]}; *(u32x4*)(bo + (1 - hh) * 512 + pos * 16) = v;
                const u32x2 tl = hh ? qd[u + 4] : qd[u + 2];
                *(u32x2*)((hh ? bo : be) + 1024 + pos * 8) = tl;
            }
        }
        if (GRP == 1 && EXTRA) {
            char* be = ws + WS_KV + (size_t)(32 * 64 + tile) * KV_B;
            u32x4 v = {qd[8][0], qd[8][1], qd[9][0], qd[9][1]}; *(u32x4*)(be + hh * 512 + pos * 16) = v;
            if (hh == 0) *(u32x2*)(be + 1024 + pos * 8) = qd[10];
        }
    } else {
#pragma unroll
        for (int t = 0; t < NT; ++t) {
            const bool ex = (GRP == 1 && t == 2);
            const int n = ex ? r32 : 32 * (3 * GRP + t) + r32; const int hl = n / 20, d = n - hl * 20;
            char* b = ws + WS_KV + ((size_t)(ex ? 32 : head0 + hl) * 64 + tile) * KV_B + 1280 + hh * VBLK + d * 16;
            if (!ex || r32 < 20) {
#pragma unroll
                for (int s = 0; s < 2; ++s) {
                    u32x4 v = {cvtpk(acc[t][8 * s], acc[t][8 * s + 1]), cvtpk(acc[t][8 * s + 2], acc[t][8 * s + 3]), cvtpk(acc[t][8 * s + 4], acc[t][8 * s + 5]), cvtpk(acc[t][8 * s + 6], acc[t][8 * s + 7])};
                    *(u32x4*)(b + s * 2 * VBLK) = v;
                }
            }
        }
        const unsigned cv = (lane & 1) ? 0u : 0x3F803F80u; const u32x4 cvec = {cv, cv, cv, cv};
        if (GRP == 0) *(u32x4*)(ws + WS_KV + ((size_t)(head0 + (lane >> 3)) * 64 + tile) * KV_B + 1280 + ((lane >> 1) & 3) * VBLK + 320 + (lane & 1) * 16) = cvec;
        if (GRP == 1 && EXTRA && lane < 8) *(u32x4*)(ws + WS_KV + ((size_t)32 * 64 + tile) * KV_B + 1280 + ((lane >> 1) & 3) * VBLK + 320 + (lane & 1) * 16) = cvec;
    }
}

__global__ __launch_bounds__(512, 2) void k1_proj(const u16* __restrict__ x, const u16* __restrict__ wqA, const u16* __restrict__ wkA, const u16* __restrict__ wvA,
                                                  const u16* __restrict__ wqkv, char* __restrict__ ws) {
    __shared__ __attribute__((aligned(16))) char lds[K1_NS * K1_SLOT];
    const int b = blockIdx.x, xcd = b & 7, loc = b >> 3;
    const int mt = (xcd & 1) * 8 + (loc & 7), type = (xcd >> 1) * 4 + (loc >> 3);
    const int cls = type >> 2, j = type & 3;
    const int lane = threadIdx.x & 63, w = __builtin_amdgcn_readfirstlane(threadIdx.x >> 6), wv = w & 3;
    const u16* wsrc = (cls == 0) ? wqA + 160 * j : wqkv + (cls - 1) * 640 + 160 * j;
    const int ldw = (cls == 0) ? 640 : 1920;
    const int kind = (cls <= 1) ? 0 : cls - 1;
    const int head0 = 8 * j + ((cls == 1) ? 32 : 0);
    const bool extra = (cls >= 2) && (j == 0);
    if (cls == 3) {
        if (w < 4) k1_body<0, true, false>(lds, x, wsrc, ldw, wvA, mt, kind, head0, ws, wv, lane);
        else if (extra) k1_body<1, true, true>(lds, x, wsrc, ldw, wvA, mt, kind, head0, ws, wv, lane);
        else k1_body<1, true, false>(lds, x, wsrc, ldw, wvA, mt, kind, head0, ws, wv, lane);
    } else {
        if (w < 4) k1_body<0, false, false>(lds, x, wsrc, ldw, wkA, mt, kind, head0, ws, wv, lane);
        else if (extra) k1_body<1, false, true>(lds, x, wsrc, ldw, wkA, mt, kind, head0, ws, wv, lane);
        else k1_body<1, false, false>(lds, x, wsrc, ldw, wkA, mt, kind, head0, ws, wv, lane);
    }
}

constexpr int K2_LDS = 163840, K2_RES = 60;

__device__ __forceinline__ f32x16 smf(const bf16x8& v, const f32x8& p, const f32x16& o, int idx) {
    return __builtin_amdgcn_smfmac_f32_32x32x32_bf16(__builtin_bit_cast(bfv8, v), __builtin_bit_cast(bfv16, p), o, idx, 0, 0);
}
#define LO8(S) __builtin_shufflevector(S, S, 0, 1, 2, 3, 4, 5, 6, 7)
#define HI8(S) __builtin_shufflevector(S, S, 8, 9, 10, 11, 12, 13, 14, 15)
#define EX(S, i) S[i] = __builtin_amdgcn_exp2f(S[i])

__device__ __forceinline__ float halfmax(float m) {
    auto rr = __builtin_amdgcn_permlane32_swap(__float_as_uint(m), __float_as_uint(m), false, false);
    return fmaxf(__uint_as_float(rr[0]), __uint_as_float(rr[1]));
}
__device__ __forceinline__ float max16(const f32x16& s) {
    float a = fmaxf(fmaxf(s[0], s[1]), s[2]), b = fmaxf(fmaxf(s[3], s[4]), s[5]);
    a = fmaxf(fmaxf(a, s[6]), s[7]); b = fmaxf(fmaxf(b, s[8]), s[9]);
    a = fmaxf(fmaxf(a, s[10]), s[11]); b = fmaxf(fmaxf(b, s[12]), s[13]);
    a = fmaxf(fmaxf(a, s[14]), s[15]);
    return halfmax(fmaxf(a, b));
}

__device__ __forceinline__ f32x16 attn_slow(const char* __restrict__ kvg, int lane, bf16x8 q0, bf16x4 q1) {
    const int r32 = lane & 31, hh = lane >> 5, vr = r32 < 21 ? r32 : 21;
    f32x16 o = f32x16{}; float m = -INFINITY;
#pragma unroll 1
    for (int t = 0; t < 64; ++t) {
        const char* b = kvg + (size_t)t * KV_B;
        const bf16x8 k0 = *(const bf16x8*)(b + lane * 16); const bf16x4 k1 = *(const bf16x4*)(b + 1024 + r32 * 8);
        const bf16x8 v0 = *(const bf16x8*)(b + 1280 + hh * VBLK + vr * 16), v1 = *(const bf16x8*)(b + 1280 + (2 + hh) * VBLK + vr * 16);
        f32x16 s = MFMA16(k0, q0, f32x16{}); s = MFMA8(k1, q1, s);
        const float mn = fmaxf(m, max16(s)); const float al = __builtin_amdgcn_exp2f(m - mn); m = mn;
#pragma unroll
        for (int i = 0; i < 16; ++i) { o[i] *= al; s[i] = __builtin_amdgcn_exp2f(s[i] - mn); }
        o = smf(v0, LO8(s), o, (int)0xDDDDDDDD); o = smf(v1, HI8(s), o, (int)0xDDDDDDDD);
    }
    return o;
}

__device__ __forceinline__ void store_o(const f32x16& o, char* __restrict__ ws, int hq, int qt, int lane) {
    const int r32 = lane & 31, hh = lane >> 5;
    auto rr = __builtin_amdgcn_permlane32_swap(__float_as_uint(o[8]), __float_as_uint(o[8]), false, false);
    const float inv = 1.0f / __uint_as_float(rr[1]);
    const int rt = 2 * qt + (r32 >> 4), P = 5 * (hq >> 1) + 2 * (hq & 1) + hh, PT = 5 * (hq >> 1) + 4;
    char* b = ws + WS_O + (size_t)rt * 40 * 1024 + (r32 & 15) * 16;
    u32x4 v = {cvtpk(o[0] * inv, o[1] * inv), cvtpk(o[2] * inv, o[3] * inv), cvtpk(o[4] * inv, o[5] * inv), cvtpk(o[6] * inv, o[7] * inv)};
    *(u32x4*)(b + P * 256) = v;
    if (hh == 0) { u32x2 t = {cvtpk(o[8] * inv, o[9] * inv), cvtpk(o[10] * inv, o[11] * inv)}; *(u32x2*)(b + PT * 256 + (hq & 1) * 8) = t; }
}
__device__ __forceinline__ bool unsafe_o(const f32x16& o) {
    float a = 0.f;
#pragma unroll
    for (int i = 0; i < 12; ++i) a = fmaxf(a, fabsf(o[i]) < 1e30f ? 0.f : 1.f);
    return a != 0.f;
}

__global__ __launch_bounds__(512, 2) void k2_attn(const u16* __restrict__ x, const u16* __restrict__ gw, const u16* __restrict__ woA, const u16* __restrict__ woB, char* __restrict__ ws) {
    __shared__ __attribute__((aligned(16))) char lds[K2_LDS];
    const int b = blockIdx.x, xcd = b & 7, i5 = b >> 3;
    const int isA = i5 >> 4, head = xcd + 8 * ((i5 & 15) >> 2), qb = i5 & 3;
    const int hq = isA ? head : 32 + head, hk = isA ? 32 : head;
    const int lane = threadIdx.x & 63, w = __builtin_amdgcn_readfirstlane(threadIdx.x >> 6);
    const int r32 = lane & 31, hh = lane >> 5;
    const unsigned lds0 = (unsigned)(uintptr_t)lds;
    const char* kvg = ws + WS_KV + (size_t)hk * 64 * KV_B;
    const int qt = 16 * qb + 2 * w;
    const char* qg = ws + WS_Q + ((size_t)hq * 64 + qt) * QT_B;
    const bf16x8 qa0 = *(const bf16x8*)(qg + lane * 16), qb0 = *(const bf16x8*)(qg + QT_B + lane * 16);
    bf16x4 qa1 = *(const bf16x4*)(qg + 1024 + r32 * 8), qb1 = *(const bf16x4*)(qg + QT_B + 1024 + r32 * 8);
    float gsum = 0.f;
    {
        const int row = 8 * b + w;
        for (int c = lane; c < 80; c += 64) {
            const u32x4 xv = *(const u32x4*)(x + (size_t)row * DM + c * 8), gv = *(const u32x4*)(gw + c * 8);
#pragma unroll
            for (int e = 0; e < 4; ++e) gsum += bfr(bf2f(xv[e] & 0xffffu) * bf2f(gv[e] & 0xffffu)) + bfr(bf2f(xv[e] >> 16) * bf2f(gv[e] >> 16));
        }
#pragma unroll
        for (int o = 32; o >= 1; o >>= 1) gsum += __shfl_xor(gsum, o);
        const float z = bfr(gsum), g = bfr(1.0f / (1.0f + __expf(-z)));
        if (lane == 0) ((float*)(ws + WS_G))[row] = g;
    }
    {
        const int item = 8 * b + w;
        if (item < 1600) {
            const int P = item / 10, n = (item - P * 10) * 64 + lane, m = P / 5, i = P - m * 5;
            unsigned e[8];
#pragma unroll
            for (int j = 0; j < 8; ++j) {
                const int hd = (i < 4) ? 2 * m + (i >> 1) : 2 * m + (j >> 2);
                const int d = (i < 4) ? (j & 3) + 8 * (j >> 2) + 4 * (i & 1) : 16 + (j & 3);
                const u16* src = (hd < 32) ? woA + (size_t)(hd * 20 + d) * DM : woB + (size_t)((hd - 32) * 20 + d) * DM;
                e[j] = src[n];
            }
            u32x4 v = {e[0] | (e[1] << 16), e[2] | (e[3] << 16), e[4] | (e[5] << 16), e[6] | (e[7] << 16)};
            *(u32x4*)(ws + WS_WO + ((size_t)(n >> 4) * 40 + (P >> 2)) * 1024 + ((P & 3) * 16 + (n & 15)) * 16) = v;
        }
    }
    if (hh) { qa1 = bf16x4{}; qb1 = bf16x4{}; }
    asm volatile("s_waitcnt vmcnt(0)" ::: "memory");
    PIN(qa1); PIN(qb1);
#pragma unroll
    for (int i = 0; i < 20; ++i) glds16(kvg + (8 * i + w) * 1024, lane * 16, lds0 + (8 * i + w) * 1024);

    const int vr = r32 < 21 ? r32 : 21;
    const lds_cptr pk0 = (lds_cptr)lds + lane * 16, pk1 = (lds_cptr)lds + 1024 + r32 * 8, pv = (lds_cptr)lds + 1280 + hh * VBLK + vr * 16;
#define LDK0(off) (*(LAS const bf16x8*)(pk0 + (off)))
#define LDK1(off) (*(LAS const bf16x4*)(pk1 + (off)))
#define LDV0(off) (*(LAS const bf16x8*)(pv + (off)))
#define LDV1(off) (*(LAS const bf16x8*)(pv + (off) + 2 * VBLK))
    int idx = (int)0xDDDDDDDD; PIN(idx);
    wait_bar<17>();
    bf16x8 k0 = LDK0(0), v0 = LDV0(0), v1 = bf16x8{}; bf16x4 k1 = LDK1(0);
    f32x16 Sa, Sb, Ca, Cb, Oa = f32x16{}, Ob = f32x16{};
    Sa = MFMA16(k0, qa0, f32x16{}); Sa = MFMA8(k1, qa1, Sa);
    Sb = MFMA16(k0, qb0, f32x16{}); Sb = MFMA8(k1, qb1, Sb);
    {
        const float ma = max16(Sa), mb = max16(Sb);
#pragma unroll
        for (int i = 0; i < 16; ++i) { Ca[i] = -ma; Cb[i] = -mb; Sa[i] -= ma; Sb[i] = 0.f; }
    }
    PIN(Ca); PIN(Cb);
    int cur = 0;
    for (int it = 0; it < 16; ++it) {
        if (it == 2) wait_bar<11>();
        else if (it == 6) {
            wait_bar<0>();
            glds16(kvg + K2_RES * KV_B + w * 1024, lane * 16, lds0 + w * 1024);
            glds16(kvg + K2_RES * KV_B + (8 + w) * 1024, lane * 16, lds0 + (8 + w) * 1024);
        } else if (it == 14) wait_bar<0>();
        const int nxt = (it == 14) ? 0 : cur + 4 * KV_B;
#pragma unroll
        for (int u = 0; u < 4; ++u) {
            const int oc = cur + u * KV_B, on = (u == 3) ? nxt : cur + (u + 1) * KV_B;
            SBAR();
            EX(Sa, 0); EX(Sa, 1); PIN(Sa); SBAR();
            Ob = smf(v1, HI8(Sb), Ob, idx); v1 = LDV1(oc); SBAR();
            EX(Sa, 2); EX(Sa, 3); EX(Sa, 4); EX(Sa, 5); PIN(Sa); SBAR();
            Sb = MFMA16(k0, qb0, Cb); k0 = LDK0(on); SBAR();
            EX(Sa, 6); EX(Sa, 7); EX(Sa, 8); EX(Sa, 9); PIN(Sa); SBAR();
            Sb = MFMA8(k1, qb1, Sb); k1 = LDK1(on); SBAR();
            EX(Sa, 10); EX(Sa, 11); EX(Sa, 12); EX(Sa, 13); PIN(Sa); SBAR();
            Oa = smf(v0, LO8(Sa), Oa, idx); SBAR();
            EX(Sa, 14); EX(Sa, 15); PIN(Sa); EX(Sb, 0); EX(Sb, 1); PIN(Sb); SBAR();
            Oa = smf(v1, HI8(Sa), Oa, idx); SBAR();
            EX(Sb, 2); EX(Sb, 3); EX(Sb, 4); EX(Sb, 5); PIN(Sb); SBAR();
            Sa = MFMA16(k0, qa0, Ca); SBAR();
            EX(Sb, 6); EX(Sb, 7); EX(Sb, 8); EX(Sb, 9); PIN(Sb); SBAR();
            Sa = MFMA8(k1, qa1, Sa); SBAR();
            EX(Sb, 10); EX(Sb, 11); EX(Sb, 12); EX(Sb, 13); PIN(Sb); SBAR();
            Ob = smf(v0, LO8(Sb), Ob, idx); v0 = LDV0(on); SBAR();
            EX(Sb, 14); EX(Sb, 15); PIN(Sb); SBAR();
        }
        cur = nxt;
    }
    Ob = smf(v1, HI8(Sb), Ob, idx);
    if (__any(unsafe_o(Oa) || unsafe_o(Ob))) { Oa = attn_slow(kvg, lane, qa0, qa1); Ob = attn_slow(kvg, lane, qb0, qb1); }
    store_o(Oa, ws, hq, qt, lane);
    store_o(Ob, ws, hq, qt + 1, lane);
}

__global__ __launch_bounds__(256, 1) void k3_out(const char* __restrict__ ws, u16* __restrict__ out) {
    __shared__ __attribute__((aligned(16))) char lds[4 * 20 * 1024];
    const int b = blockIdx.x, xcd = b & 7, loc = b >> 3;
    const int mt = (xcd >> 1) * 8 + (loc & 7), nb = (xcd & 1) * 4 + (loc >> 3);
    const int lane = threadIdx.x & 63, w = __builtin_amdgcn_readfirstlane(threadIdx.x >> 6);
    const char* og = ws + WS_O + ((size_t)(4 * mt) * 40 + 10 * w) * 1024 + lane * 16;
    const char* wg = ws + WS_WO + ((size_t)(5 * nb) * 40 + 10 * w) * 1024 + lane * 16;
    f32x4 acc[4][5];
#pragma unroll
    for (int i = 0; i < 4; ++i)
#pragma unroll
        for (int j = 0; j < 5; ++j) acc[i][j] = f32x4{};
    bf16x8 af[10][4], wf[10][5];
#pragma unroll
    for (int ks = 0; ks < 10; ++ks) {
#pragma unroll
        for (int i = 0; i < 4; ++i) af[ks][i] = *(const bf16x8*)(og + (size_t)(i * 40 + ks) * 1024);
#pragma unroll
        for (int j = 0; j < 5; ++j) wf[ks][j] = *(const bf16x8*)(wg + (size_t)(j * 40 + ks) * 1024);
    }
    const float g = ((const float*)(ws + WS_G))[64 * mt + 16 * w + (lane & 15)];
#pragma unroll
    for (int ks = 0; ks < 10; ++ks)
#pragma unroll
        for (int i = 0; i < 4; ++i)
#pragma unroll
            for (int j = 0; j < 5; ++j)
                acc[i][j] = __builtin_amdgcn_mfma_f32_16x16x32_bf16(__builtin_bit_cast(bfv8, wf[ks][j]), __builtin_bit_cast(bfv8, af[ks][i]), acc[i][j], 0, 0, 0);
#pragma unroll
    for (int i = 0; i < 4; ++i)
#pragma unroll
        for (int j = 0; j < 5; ++j) *(f32x4*)(lds + ((w * 4 + i) * 5 + j) * 1024 + lane * 16) = acc[i][j];
    __syncthreads();
    const float g1 = bfr(1.0f - g);
    u16* orow = out + (size_t)(64 * mt + 16 * w + (lane & 15)) * DM + 80 * nb + 4 * (lane >> 4);
#pragma unroll
    for (int j = 0; j < 5; ++j) {
        const f32x4 a0 = *(const f32x4*)(lds + ((0 * 4 + w) * 5 + j) * 1024 + lane * 16), a1 = *(const f32x4*)(lds + ((1 * 4 + w) * 5 + j) * 1024 + lane * 16);
        const f32x4 b0 = *(const f32x4*)(lds + ((2 * 4 + w) * 5 + j) * 1024 + lane * 16), b1 = *(const f32x4*)(lds + ((3 * 4 + w) * 5 + j) * 1024 + lane * 16);
        float r[4];
#pragma unroll
        for (int e = 0; e < 4; ++e) r[e] = bfr(g * bfr(a0[e] + a1[e])) + bfr(g1 * bfr(b0[e] + b1[e]));
        u32x2 v = {cvtpk(r[0], r[1]), cvtpk(r[2], r[3])};
        *(u32x2*)(orow + 16 * j) = v;
    }
}

extern "C" void kernel_launch(void* const* d_in, const int* in_sizes, int n_in, void* d_out, int out_size, void* d_ws, size_t ws_size, hipStream_t stream) {
    const u16* x = (const u16*)d_in[0];
    const u16* wqA = (const u16*)d_in[1];
    const u16* wkA = (const u16*)d_in[2];
    const u16* wvA = (const u16*)d_in[3];
    const u16* woA = (const u16*)d_in[4];
    const u16* wqkv = (const u16*)d_in[6];
    const u16* woB = (const u16*)d_in[7];
    const u16* gw = (const u16*)d_in[8];
    char* ws = (char*)d_ws;
    k1_proj<<<256, 512, 0, stream>>>(x, wqA, wkA, wvA, wqkv, ws);
    k2_attn<<<256, 512, 0, stream>>>(x, gw, woA, woB, ws);
    k3_out<<<256, 256, 0, stream>>>(ws, (u16*)d_out);
}
```

```cpp
#include <hip/hip_runtime.h>
#include <hip/hip_bf16.h>
#include <stdint.h>

typedef short bf16x8 __attribute__((ext_vector_type(8)));
typedef short bf16x4 __attribute__((ext_vector_type(4)));
typedef __bf16 bfv8 __attribute__((ext_vector_type(8)));
typedef __bf16 bfv16 __attribute__((ext_vector_type(16)));
typedef float f32x16 __attribute__((ext_vector_type(16)));
typedef float f32x8 __attribute__((ext_vector_type(8)));
typedef float f32x4 __attribute__((ext_vector_type(4)));
typedef unsigned u32x4 __attribute__((ext_vector_type(4)));
typedef unsigned u32x2 __attribute__((ext_vector_type(2)));
typedef unsigned short u16;
#define LAS __attribute__((address_space(3)))
typedef LAS const char* lds_cptr;

constexpr int SEQ = 2048, DM = 640;
constexpr int QT_B = 1280;
constexpr int VROWS = 22;
constexpr int VBLK = VROWS * 16;
constexpr int KV_B = 1280 + 4 * VBLK;
constexpr size_t WS_Q = 0;
constexpr size_t WS_KV = WS_Q + (size_t)64 * 64 * QT_B;
constexpr size_t WS_O = WS_KV + (size_t)33 * 64 * KV_B + 16384;
constexpr size_t WS_WO = WS_O + (size_t)128 * 40 * 1024;
constexpr size_t WS_G = WS_WO + (size_t)40 * 40 * 1024;
constexpr float QSCALE = 0.22360679774997896f * 1.4426950408889634f;

#define MFMA16(a, b, c) __builtin_amdgcn_mfma_f32_32x32x16_bf16(__builtin_bit_cast(bfv8, a), __builtin_bit_cast(bfv8, b), c, 0, 0, 0)
#define MFMA8(a, b, c) __builtin_amdgcn_mfma_f32_32x32x8bf16_1k(a, b, c, 0, 0, 0)

#define STAMP_DECL
#define STAMP(i)
#define STAMP_FLUSH(k)
#define SBAR() __builtin_amdgcn_sched_barrier(0)
#define PIN(x) asm volatile("" : "+v"(x))

__device__ __forceinline__ unsigned cvtpk(float lo, float hi) {
    typedef __bf16 bf2 __attribute__((ext_vector_type(2)));
    bf2 v; v[0] = (__bf16)lo; v[1] = (__bf16)hi;
    return __builtin_bit_cast(unsigned, v);
}
__device__ __forceinline__ float bf2f(unsigned v16) { return __uint_as_float(v16 << 16); }
__device__ __forceinline__ float bfr(float x) { return bf2f(cvtpk(x, 0.f) & 0xffffu); }

__device__ __forceinline__ void glds16(const void* sbase, unsigned voff, unsigned lds_dst) {
    unsigned sv;
    asm volatile("s_mov_b32 %0, m0\n\ts_mov_b32 m0, %3\n\ts_nop 0\n\tglobal_load_lds_dwordx4 %1, %2\n\ts_mov_b32 m0, %0"
                 : "=&s"(sv) : "v"(voff), "s"(sbase), "s"(lds_dst) : "memory");
}
__device__ __forceinline__ void glds16_40(const void* sbase, unsigned voff, unsigned lds_dst) {
    unsigned sv;
    asm volatile("s_mov_b32 %0, m0\n\ts_mov_b32 m0, %3\n\ts_mov_b32 exec_hi, 0xff\n\tglobal_load_lds_dwordx4 %1, %2\n\ts_mov_b32 exec_hi, -1\n\ts_mov_b32 m0, %0"
                 : "=&s"(sv) : "v"(voff), "s"(sbase), "s"(lds_dst) : "memory");
}
template <int N> __device__ __forceinline__ void wait_bar() {
    asm volatile("s_waitcnt vmcnt(%0) lgkmcnt(0)\n\ts_barrier" :: "n"(N) : "memory");
}
typedef short v4i16_t __attribute__((ext_vector_type(4)));
__device__ __forceinline__ bf16x4 ldtr(lds_cptr p) { return __builtin_amdgcn_ds_read_tr16_b64_v4i16((LAS v4i16_t*)p); }

constexpr int K1_X = 0, K1_W = 16384, K1_E = 36864, K1_SLOT = 39488, K1_NS = 4;

template <int GRP, bool VOR, bool EXTRA>
__device__ __forceinline__ void k1_body(char* lds, const u16* __restrict__ x, const u16* __restrict__ wsrc, int ldw, const u16* __restrict__ esrc,
                                        int mt, int kind  , int head0, char* __restrict__ ws, int wv, int lane) {
    constexpr int NT = (GRP == 0) ? 3 : (EXTRA ? 3 : 2);
    constexpr int CNT = (GRP == 0) ? 5 : (EXTRA ? 5 : 4);
    STAMP_DECL; STAMP(0);
    const unsigned lds0 = (unsigned)(uintptr_t)lds;
    const int r32 = lane & 31, hh = lane >> 5;
    unsigned voff[5];
    if (GRP == 0) {
#pragma unroll
        for (int i = 0; i < 5; ++i) { const int id = 64 * (wv + 4 * i) + lane; const int row = id / 20, c = id - row * 20; voff[i] = (unsigned)(row * ldw * 2 + 16 * c); }
    } else {
#pragma unroll
        for (int i = 0; i < 4; ++i) { const int row = 8 * (wv + 4 * i) + (lane >> 3); const int c = (lane & 7) ^ ((row >> 1) & 7); voff[i] = (unsigned)((128 * mt + row) * (DM * 2) + 16 * c); }
        voff[4] = (unsigned)(wv * 640 + lane * 16);
    }
    auto issue = [&](int s) {
        const unsigned slot = lds0 + (s % K1_NS) * K1_SLOT;
        if (GRP == 0) {
            const char* b = (const char*)wsrc + (size_t)s * 64 * ldw * 2;
#pragma unroll
            for (int i = 0; i < 5; ++i) glds16(b, voff[i], slot + K1_W + 1024 * (wv + 4 * i));
        } else {
            const char* b = (const char*)x + s * 128;
#pragma unroll
            for (int i = 0; i < 4; ++i) glds16(b, voff[i], slot + K1_X + 1024 * (wv + 4 * i));
            if (EXTRA) glds16_40((const char*)esrc + s * 2560, voff[4], slot + K1_E + wv * 640);
        }
    };
    issue(0); issue(1); issue(2); issue(3);
    STAMP(1);

    f32x16 acc[NT];
#pragma unroll
    for (int t = 0; t < NT; ++t) acc[t] = f32x16{};
    const int sw = (r32 >> 1) & 7;
    const lds_cptr xb = (lds_cptr)lds + K1_X + wv * 4096 + r32 * 128;
    const int q4 = (lane & 15) >> 2, p4 = lane & 3, blk = (lane >> 4) & 1;
    const lds_cptr wb = (lds_cptr)lds + K1_W + (8 * hh + q4) * 320 + blk * 32 + p4 * 8 + GRP * 192;
    const lds_cptr eb = (lds_cptr)lds + K1_E + (8 * hh + q4) * 40 + blk * 32 + p4 * 8;
#pragma unroll
    for (int s = 0; s < 10; ++s) {
        if (s == 0) wait_bar<3 * CNT>(); else if (s <= 7) wait_bar<2 * CNT>(); else if (s == 8) wait_bar<CNT>(); else wait_bar<0>();
        if (s == 0) STAMP(2);
        if (s == 4) STAMP(3);
        if (s >= 1 && s + 3 < 10) issue(s + 3);
        const int so = (s % K1_NS) * K1_SLOT;
#pragma unroll
        for (int kk = 0; kk < 4; ++kk) {
            const bf16x8 xf = *(LAS const bf16x8*)(xb + so + 16 * ((2 * kk + hh) ^ sw));
#pragma unroll
            for (int t = 0; t < NT; ++t) {
                bf16x4 lo, hi;
                if (GRP == 1 && t == 2) { lo = ldtr(eb + so + kk * 640); hi = ldtr(eb + so + kk * 640 + 160); }
                else { lo = ldtr(wb + so + kk * 5120 + t * 64); hi = ldtr(wb + so + kk * 5120 + 1280 + t * 64); }
                const bf16x8 wf = __builtin_shufflevector(lo, hi, 0, 1, 2, 3, 4, 5, 6, 7);
                acc[t] = VOR ? MFMA16(xf, wf, acc[t]) : MFMA16(wf, xf, acc[t]);
            }
        }
    }
    STAMP(4);
    const int tile = 4 * mt + wv;
    if (!VOR) {
        const float sc = (kind == 0) ? QSCALE : 1.0f;
        const int pos = (kind == 0) ? r32 : ((r32 & 19) | ((r32 & 4) << 1) | ((r32 & 8) >> 1));
        const size_t hstride = (kind == 0) ? (size_t)64 * QT_B : (size_t)64 * KV_B;
        char* base = ws + ((kind == 0) ? WS_Q + (size_t)tile * QT_B : WS_KV + (size_t)tile * KV_B) + (size_t)head0 * hstride;
        u32x2 qd[NT * 4];
#pragma unroll
        for (int t = 0; t < NT; ++t)
#pragma unroll
            for (int g = 0; g < 4; ++g) { qd[t * 4 + g][0] = cvtpk(acc[t][4 * g] * sc, acc[t][4 * g + 1] * sc); qd[t * 4 + g][1] = cvtpk(acc[t][4 * g + 2] * sc, acc[t][4 * g + 3] * sc); }
        constexpr int U0 = GRP * 12;
#pragma unroll
        for (int m = 0; m < 4; ++m) {
            char* be = base + (size_t)(2 * m) * hstride; char* bo = be + hstride;
            const int u = 5 * m - U0;
            if (u >= 0 && u + 1 < ((GRP == 0) ? 12 : 8)) { u32x4 v = {qd[u][0], qd[u][1], qd[u + 1][0], qd[u + 1][1]}; *(u32x4*)(be + hh * 512 + pos * 16) = v; }
            if (u + 2 >= 0 && u + 4 < ((GRP == 0) ? 12 : 8)) {
                const u32x2 a = hh ? qd[u + 2] : qd[u + 3], b = hh ? qd[u + 3] : qd[u + 4];
                u32x4 v = {a[0], a[1], b[0], b[1]}; *(u32x4*)(bo + (1 - hh) * 512 + pos * 16) = v;
                const u32x2 tl = hh ? qd[u + 4] : qd[u + 2];
                *(u32x2*)((hh ? bo : be) + 1024 + pos * 8) = tl;
            }
        }
        if (GRP == 1 && EXTRA) {
            char* be = ws + WS_KV + (size_t)(32 * 64 + tile) * KV_B;
            u32x4 v = {qd[8][0], qd[8][1], qd[9][0], qd[9][1]}; *(u32x4*)(be + hh * 512 + pos * 16) = v;
            if (hh == 0) *(u32x2*)(be + 1024 + pos * 8) = qd[10];
        }
    } else {
#pragma unroll
        for (int t = 0; t < NT; ++t) {
            const bool ex = (GRP == 1 && t == 2);
            const int n = ex ? r32 : 32 * (3 * GRP + t) + r32; const int hl = n / 20, d = n - hl * 20;
            char* b = ws + WS_KV + ((size_t)(ex ? 32 : head0 + hl) * 64 + tile) * KV_B + 1280 + hh * VBLK + d * 16;
            if (!ex || r32 < 20) {
#pragma unroll
                for (int s = 0; s < 2; ++s) {
                    u32x4 v = {cvtpk(acc[t][8 * s], acc[t][8 * s + 1]), cvtpk(acc[t][8 * s + 2], acc[t][8 * s + 3]), cvtpk(acc[t][8 * s + 4], acc[t][8 * s + 5]), cvtpk(acc[t][8 * s + 6], acc[t][8 * s + 7])};
                    *(u32x4*)(b + s * 2 * VBLK) = v;
                }
            }
        }
        const unsigned cv = (lane & 1) ? 0u : 0x3F803F80u; const u32x4 cvec = {cv, cv, cv, cv};
        if (GRP == 0) *(u32x4*)(ws + WS_KV + ((size_t)(head0 + (lane >> 3)) * 64 + tile) * KV_B + 1280 + ((lane >> 1) & 3) * VBLK + 320 + (lane & 1) * 16) = cvec;
        if (GRP == 1 && EXTRA && lane < 8) *(u32x4*)(ws + WS_KV + ((size_t)32 * 64 + tile) * KV_B + 1280 + ((lane >> 1) & 3) * VBLK + 320 + (lane & 1) * 16) = cvec;
    }
    STAMP(5);
    STAMP(6); STAMP_FLUSH(0);
}

__global__ __launch_bounds__(512, 2) void k1_proj(const u16* __restrict__ x, const u16* __restrict__ wqA, const u16* __restrict__ wkA, const u16* __restrict__ wvA,
                                                  const u16* __restrict__ wqkv, char* __restrict__ ws) {
    __shared__ __attribute__((aligned(16))) char lds[K1_NS * K1_SLOT];
    const int b = blockIdx.x, xcd = b & 7, loc = b >> 3;
    const int mt = (xcd & 1) * 8 + (loc & 7), type = (xcd >> 1) * 4 + (loc >> 3);
    const int cls = type >> 2, j = type & 3;
    const int lane = threadIdx.x & 63, w = __builtin_amdgcn_readfirstlane(threadIdx.x >> 6), wv = w & 3;
    const u16* wsrc = (cls == 0) ? wqA + 160 * j : wqkv + (cls - 1) * 640 + 160 * j;
    const int ldw = (cls == 0) ? 640 : 1920;
    const int kind = (cls <= 1) ? 0 : cls - 1;
    const int head0 = 8 * j + ((cls == 1) ? 32 : 0);
    const bool extra = (cls >= 2) && (j == 0);
    if (cls == 3) {
        if (w < 4) k1_body<0, true, false>(lds, x, wsrc, ldw, wvA, mt, kind, head0, ws, wv, lane);
        else if (extra) k1_body<1, true, true>(lds, x, wsrc, ldw, wvA, mt, kind, head0, ws, wv, lane);
        else k1_body<1, true, false>(lds, x, wsrc, ldw, wvA, mt, kind, head0, ws, wv, lane);
    } else {
        if (w < 4) k1_body<0, false, false>(lds, x, wsrc, ldw, wkA, mt, kind, head0, ws, wv, lane);
        else if (extra) k1_body<1, false, true>(lds, x, wsrc, ldw, wkA, mt, kind, head0, ws, wv, lane);
        else k1_body<1, false, false>(lds, x, wsrc, ldw, wkA, mt, kind, head0, ws, wv, lane);
    }
}

constexpr int K2_LDS = 163840, K2_RES = 60;

__device__ __forceinline__ f32x16 smf(const bf16x8& v, const f32x8& p, const f32x16& o, int idx) {
    return __builtin_amdgcn_smfmac_f32_32x32x32_bf16(__builtin_bit_cast(bfv8, v), __builtin_bit_cast(bfv16, p), o, idx, 0, 0);
}
#define LO8(S) __builtin_shufflevector(S, S, 0, 1, 2, 3, 4, 5, 6, 7)
#define HI8(S) __builtin_shufflevector(S, S, 8, 9, 10, 11, 12, 13, 14, 15)
#define EX(S, i) S[i] = __builtin_amdgcn_exp2f(S[i])

__device__ __forceinline__ float halfmax(float m) {
    auto rr = __builtin_amdgcn_permlane32_swap(__float_as_uint(m), __float_as_uint(m), false, false);
    return fmaxf(__uint_as_float(rr[0]), __uint_as_float(rr[1]));
}
__device__ __forceinline__ float max16(const f32x16& s) {
    float a = fmaxf(fmaxf(s[0], s[1]), s[2]), b = fmaxf(fmaxf(s[3], s[4]), s[5]);
    a = fmaxf(fmaxf(a, s[6]), s[7]); b = fmaxf(fmaxf(b, s[8]), s[9]);
    a = fmaxf(fmaxf(a, s[10]), s[11]); b = fmaxf(fmaxf(b, s[12]), s[13]);
    a = fmaxf(fmaxf(a, s[14]), s[15]);
    return halfmax(fmaxf(a, b));
}

__device__ __forceinline__ f32x16 attn_slow(const char* __restrict__ kvg, int lane, bf16x8 q0, bf16x4 q1) {
    const int r32 = lane & 31, hh = lane >> 5, vr = r32 < 21 ? r32 : 21;
    f32x16 o = f32x16{}; float m = -INFINITY;
#pragma unroll 1
    for (int t = 0; t < 64; ++t) {
        const char* b = kvg + (size_t)t * KV_B;
        const bf16x8 k0 = *(const bf16x8*)(b + lane * 16); const bf16x4 k1 = *(const bf16x4*)(b + 1024 + r32 * 8);
        const bf16x8 v0 = *(const bf16x8*)(b + 1280 + hh * VBLK + vr * 16), v1 = *(const bf16x8*)(b + 1280 + (2 + hh) * VBLK + vr * 16);
        f32x16 s = MFMA16(k0, q0, f32x16{}); s = MFMA8(k1, q1, s);
        const float mn = fmaxf(m, max16(s)); const float al = __builtin_amdgcn_exp2f(m - mn); m = mn;
#pragma unroll
        for (int i = 0; i < 16; ++i) { o[i] *= al; s[i] = __builtin_amdgcn_exp2f(s[i] - mn); }
        o = smf(v0, LO8(s), o, (int)0xDDDDDDDD); o = smf(v1, HI8(s), o, (int)0xDDDDDDDD);
    }
    return o;
}

__device__ __forceinline__ void store_o(const f32x16& o, char* __restrict__ ws, int hq, int qt, int lane) {
    const int r32 = lane & 31, hh = lane >> 5;
    auto rr = __builtin_amdgcn_permlane32_swap(__float_as_uint(o[8]), __float_as_uint(o[8]), false, false);
    const float inv = 1.0f / __uint_as_float(rr[1]);
    const int rt = 2 * qt + (r32 >> 4), P = 5 * (hq >> 1) + 2 * (hq & 1) + hh, PT = 5 * (hq >> 1) + 4;
    char* b = ws + WS_O + (size_t)rt * 40 * 1024 + (r32 & 15) * 16;
    u32x4 v = {cvtpk(o[0] * inv, o[1] * inv), cvtpk(o[2] * inv, o[3] * inv), cvtpk(o[4] * inv, o[5] * inv), cvtpk(o[6] * inv, o[7] * inv)};
    *(u32x4*)(b + P * 256) = v;
    if (hh == 0) { u32x2 t = {cvtpk(o[8] * inv, o[9] * inv), cvtpk(o[10] * inv, o[11] * inv)}; *(u32x2*)(b + PT * 256 + (hq & 1) * 8) = t; }
}
__device__ __forceinline__ bool unsafe_o(const f32x16& o) {
    float a = 0.f;
#pragma unroll
    for (int i = 0; i < 12; ++i) a = fmaxf(a, fabsf(o[i]) < 1e30f ? 0.f : 1.f);
    return a != 0.f;
}

__global__ __launch_bounds__(512, 2) void k2_attn(const u16* __restrict__ x, const u16* __restrict__ gw, const u16* __restrict__ woA, const u16* __restrict__ woB, char* __restrict__ ws) {
    __shared__ __attribute__((aligned(16))) char lds[K2_LDS];
    const int b = blockIdx.x, xcd = b & 7, i5 = b >> 3;
    const int isA = i5 >> 4, head = xcd + 8 * ((i5 & 15) >> 2), qb = i5 & 3;
    const int hq = isA ? head : 32 + head, hk = isA ? 32 : head;
    const int lane = threadIdx.x & 63, w = __builtin_amdgcn_readfirstlane(threadIdx.x >> 6);
    const int r32 = lane & 31, hh = lane >> 5;
    const unsigned lds0 = (unsigned)(uintptr_t)lds;
    const char* kvg = ws + WS_KV + (size_t)hk * 64 * KV_B;
    const int qt = 16 * qb + 2 * w;
    STAMP_DECL; STAMP(0);
    const char* qg = ws + WS_Q + ((size_t)hq * 64 + qt) * QT_B;
    const bf16x8 qa0 = *(const bf16x8*)(qg + lane * 16), qb0 = *(const bf16x8*)(qg + QT_B + lane * 16);
    bf16x4 qa1 = *(const bf16x4*)(qg + 1024 + r32 * 8), qb1 = *(const bf16x4*)(qg + QT_B + 1024 + r32 * 8);
    float gsum = 0.f;
    {
        const int row = 8 * b + w;
        for (int c = lane; c < 80; c += 64) {
            const u32x4 xv = *(const u32x4*)(x + (size_t)row * DM + c * 8), gv = *(const u32x4*)(gw + c * 8);
#pragma unroll
            for (int e = 0; e < 4; ++e) gsum += bfr(bf2f(xv[e] & 0xffffu) * bf2f(gv[e] & 0xffffu)) + bfr(bf2f(xv[e] >> 16) * bf2f(gv[e] >> 16));
        }
#pragma unroll
        for (int o = 32; o >= 1; o >>= 1) gsum += __shfl_xor(gsum, o);
        const float z = bfr(gsum), g = bfr(1.0f / (1.0f + __expf(-z)));
        if (lane == 0) ((float*)(ws + WS_G))[row] = g;
    }
    {
        const int item = 8 * b + w;
        if (item < 1600) {
            const int P = item / 10, n = (item - P * 10) * 64 + lane, m = P / 5, i = P - m * 5;
            unsigned e[8];
#pragma unroll
            for (int j = 0; j < 8; ++j) {
                const int hd = (i < 4) ? 2 * m + (i >> 1) : 2 * m + (j >> 2);
                const int d = (i < 4) ? (j & 3) + 8 * (j >> 2) + 4 * (i & 1) : 16 + (j & 3);
                const u16* src = (hd < 32) ? woA + (size_t)(hd * 20 + d) * DM : woB + (size_t)((hd - 32) * 20 + d) * DM;
                e[j] = src[n];
            }
            u32x4 v = {e[0] | (e[1] << 16), e[2] | (e[3] << 16), e[4] | (e[5] << 16), e[6] | (e[7] << 16)};
            *(u32x4*)(ws + WS_WO + ((size_t)(n >> 4) * 40 + (P >> 2)) * 1024 + ((P & 3) * 16 + (n & 15)) * 16) = v;
        }
    }
    if (hh) { qa1 = bf16x4{}; qb1 = bf16x4{}; }
    asm volatile("s_waitcnt vmcnt(0)" ::: "memory");
    PIN(qa1); PIN(qb1);
    STAMP(1);
#pragma unroll
    for (int i = 0; i < 20; ++i) glds16(kvg + (8 * i + w) * 1024, lane * 16, lds0 + (8 * i + w) * 1024);

    const int vr = r32 < 21 ? r32 : 21;
    const lds_cptr pk0 = (lds_cptr)lds + lane * 16, pk1 = (lds_cptr)lds + 1024 + r32 * 8, pv = (lds_cptr)lds + 1280 + hh * VBLK + vr * 16;
#define LDK0(off) (*(LAS const bf16x8*)(pk0 + (off)))
#define LDK1(off) (*(LAS const bf16x4*)(pk1 + (off)))
#define LDV0(off) (*(LAS const bf16x8*)(pv + (off)))
#define LDV1(off) (*(LAS const bf16x8*)(pv + (off) + 2 * VBLK))
    int idx = (int)0xDDDDDDDD; PIN(idx);
    STAMP(2);
    wait_bar<17>();
    STAMP(3);
    bf16x8 k0 = LDK0(0), v0 = LDV0(0), v1 = bf16x8{}; bf16x4 k1 = LDK1(0);
    f32x16 Sa, Sb, Ca, Cb, Oa = f32x16{}, Ob = f32x16{};
    Sa = MFMA16(k0, qa0, f32x16{}); Sa = MFMA8(k1, qa1, Sa);
    Sb = MFMA16(k0, qb0, f32x16{}); Sb = MFMA8(k1, qb1, Sb);
    {
        const float ma = max16(Sa), mb = max16(Sb);
#pragma unroll
        for (int i = 0; i < 16; ++i) { Ca[i] = -ma; Cb[i] = -mb; Sa[i] -= ma; Sb[i] = 0.f; }
    }
    PIN(Ca); PIN(Cb);
    STAMP(4);
    int cur = 0;
    for (int it = 0; it < 16; ++it) {
        if (it == 2) wait_bar<11>();
        else if (it == 6) {
            wait_bar<0>();
            glds16(kvg + K2_RES * KV_B + w * 1024, lane * 16, lds0 + w * 1024);
            glds16(kvg + K2_RES * KV_B + (8 + w) * 1024, lane * 16, lds0 + (8 + w) * 1024);
        } else if (it == 14) wait_bar<0>();
        const int nxt = (it == 14) ? 0 : cur + 4 * KV_B;
#pragma unroll
        for (int u = 0; u < 4; ++u) {
            const int oc = cur + u * KV_B, on = (u == 3) ? nxt : cur + (u + 1) * KV_B;
            SBAR();
            EX(Sa, 0); EX(Sa, 1); PIN(Sa); SBAR();
            Ob = smf(v1, HI8(Sb), Ob, idx); v1 = LDV1(oc); SBAR();
            EX(Sa, 2); EX(Sa, 3); EX(Sa, 4); EX(Sa, 5); PIN(Sa); SBAR();
            Sb = MFMA16(k0, qb0, Cb); k0 = LDK0(on); SBAR();
            EX(Sa, 6); EX(Sa, 7); EX(Sa, 8); EX(Sa, 9); PIN(Sa); SBAR();
            Sb = MFMA8(k1, qb1, Sb); k1 = LDK1(on); SBAR();
            EX(Sa, 10); EX(Sa, 11); EX(Sa, 12); EX(Sa, 13); PIN(Sa); SBAR();
            Oa = smf(v0, LO8(Sa), Oa, idx); SBAR();
            EX(Sa, 14); EX(Sa, 15); PIN(Sa); EX(Sb, 0); EX(Sb, 1); PIN(Sb); SBAR();
            Oa = smf(v1, HI8(Sa), Oa, idx); SBAR();
            EX(Sb, 2); EX(Sb, 3); EX(Sb, 4); EX(Sb, 5); PIN(Sb); SBAR();
            Sa = MFMA16(k0, qa0, Ca); SBAR();
            EX(Sb, 6); EX(Sb, 7); EX(Sb, 8); EX(Sb, 9); PIN(Sb); SBAR();
            Sa = MFMA8(k1, qa1, Sa); SBAR();
            EX(Sb, 10); EX(Sb, 11); EX(Sb, 12); EX(Sb, 13); PIN(Sb); SBAR();
            Ob = smf(v0, LO8(Sb), Ob, idx); v0 = LDV0(on); SBAR();
            EX(Sb, 14); EX(Sb, 15); PIN(Sb); SBAR();
        }
        cur = nxt;
    }
    STAMP(5);
    Ob = smf(v1, HI8(Sb), Ob, idx);
    if (__any(unsafe_o(Oa) || unsafe_o(Ob))) { Oa = attn_slow(kvg, lane, qa0, qa1); Ob = attn_slow(kvg, lane, qb0, qb1); }
    store_o(Oa, ws, hq, qt, lane);
    store_o(Ob, ws, hq, qt + 1, lane);
    STAMP(6);
    STAMP(7); STAMP_FLUSH(1);
}

__global__ __launch_bounds__(256, 1) void k3_out(const char* __restrict__ ws, u16* __restrict__ out) {
    __shared__ __attribute__((aligned(16))) char lds[4 * 20 * 1024];
    const int b = blockIdx.x, xcd = b & 7, loc = b >> 3;
    const int mt = (xcd >> 1) * 8 + (loc & 7), nb = (xcd & 1) * 4 + (loc >> 3);
    const int lane = threadIdx.x & 63, w = __builtin_amdgcn_readfirstlane(threadIdx.x >> 6);
    const char* og = ws + WS_O + ((size_t)(4 * mt) * 40 + 10 * w) * 1024 + lane * 16;
    const char* wg = ws + WS_WO + ((size_t)(5 * nb) * 40 + 10 * w) * 1024 + lane * 16;
    STAMP_DECL; STAMP(0);
    f32x4 acc[4][5];
#pragma unroll
    for (int i = 0; i < 4; ++i)
#pragma unroll
        for (int j = 0; j < 5; ++j) acc[i][j] = f32x4{};
    bf16x8 af[10][4], wf[10][5];
#pragma unroll
    for (int ks = 0; ks < 10; ++ks) {
#pragma unroll
        for (int i = 0; i < 4; ++i) af[ks][i] = *(const bf16x8*)(og + (size_t)(i * 40 + ks) * 1024);
#pragma unroll
        for (int j = 0; j < 5; ++j) wf[ks][j] = *(const bf16x8*)(wg + (size_t)(j * 40 + ks) * 1024);
    }
    const float g = ((const float*)(ws + WS_G))[64 * mt + 16 * w + (lane & 15)];
    STAMP(1);
#pragma unroll
    for (int ks = 0; ks < 10; ++ks)
#pragma unroll
        for (int i = 0; i < 4; ++i)
#pragma unroll
            for (int j = 0; j < 5; ++j)
                acc[i][j] = __builtin_amdgcn_mfma_f32_16x16x32_bf16(__builtin_bit_cast(bfv8, wf[ks][j]), __builtin_bit_cast(bfv8, af[ks][i]), acc[i][j], 0, 0, 0);
#pragma unroll
    for (int i = 0; i < 4; ++i)
#pragma unroll
        for (int j = 0; j < 5; ++j) *(f32x4*)(lds + ((w * 4 + i) * 5 + j) * 1024 + lane * 16) = acc[i][j];
    STAMP(2);
    __syncthreads();
    STAMP(3);
    const float g1 = bfr(1.0f - g);
    u16* orow = out + (size_t)(64 * mt + 16 * w + (lane & 15)) * DM + 80 * nb + 4 * (lane >> 4);
#pragma unroll
    for (int j = 0; j < 5; ++j) {
        const f32x4 a0 = *(const f32x4*)(lds + ((0 * 4 + w) * 5 + j) * 1024 + lane * 16), a1 = *(const f32x4*)(lds + ((1 * 4 + w) * 5 + j) * 1024 + lane * 16);
        const f32x4 b0 = *(const f32x4*)(lds + ((2 * 4 + w) * 5 + j) * 1024 + lane * 16), b1 = *(const f32x4*)(lds + ((3 * 4 + w) * 5 + j) * 1024 + lane * 16);
        float r[4];
#pragma unroll
        for (int e = 0; e < 4; ++e) r[e] = bfr(g * bfr(a0[e] + a1[e])) + bfr(g1 * bfr(b0[e] + b1[e]));
        u32x2 v = {cvtpk(r[0], r[1]), cvtpk(r[2], r[3])};
        *(u32x2*)(orow + 16 * j) = v;
    }
    STAMP(4);
    STAMP(5); STAMP_FLUSH(2);
}

extern "C" void kernel_launch(void* const* d_in, const int* in_sizes, int n_in, void* d_out, int out_size, void* d_ws, size_t ws_size, hipStream_t stream) {
    const u16* x = (const u16*)d_in[0];
    const u16* wqA = (const u16*)d_in[1];
    const u16* wkA = (const u16*)d_in[2];
    const u16* wvA = (const u16*)d_in[3];
    const u16* woA = (const u16*)d_in[4];
    const u16* wqkv = (const u16*)d_in[6];
    const u16* woB = (const u16*)d_in[7];
    const u16* gw = (const u16*)d_in[8];
    char* ws = (char*)d_ws;
    k1_proj<<<256, 512, 0, stream>>>(x, wqA, wkA, wvA, wqkv, ws);
    k2_attn<<<256, 512, 0, stream>>>(x, gw, woA, woB, ws);
    k3_out<<<256, 256, 0, stream>>>(ws, (u16*)d_out);
}
```

```cpp
#include <hip/hip_runtime.h>
#include <hip/hip_bf16.h>
#include <stdint.h>

typedef short bf16x8 __attribute__((ext_vector_type(8)));
typedef short bf16x4 __attribute__((ext_vector_type(4)));
typedef __bf16 bfv8 __attribute__((ext_vector_type(8)));
typedef __bf16 bfv16 __attribute__((ext_vector_type(16)));
typedef float f32x16 __attribute__((ext_vector_type(16)));
typedef float f32x8 __attribute__((ext_vector_type(8)));
typedef float f32x4 __attribute__((ext_vector_type(4)));
typedef unsigned u32x4 __attribute__((ext_vector_type(4)));
typedef unsigned u32x2 __attribute__((ext_vector_type(2)));
typedef unsigned short u16;
#define LAS __attribute__((address_space(3)))
typedef LAS const char* lds_cptr;
typedef __attribute__((address_space(1))) unsigned gu32;

constexpr int SEQ = 2048, DM = 640;
constexpr int QT_B = 1280;
constexpr int KV_B = 2560;
#define VOFF(d, blk) (1280 + ((d) >> 2) * 256 + (((blk) ^ ((d) >> 2)) & 1) * 128 + ((blk) >> 1) * 64 + ((d) & 3) * 16)
constexpr size_t WS_Q = 0;
constexpr size_t WS_KV = WS_Q + (size_t)64 * 64 * QT_B;
constexpr size_t WS_O = WS_KV + (size_t)33 * 64 * KV_B;
constexpr size_t WS_WO = WS_O + (size_t)128 * 40 * 1024;
constexpr size_t WS_G = WS_WO + (size_t)40 * 40 * 1024;
constexpr size_t WS_FLAG = WS_G + 8192;
constexpr float QSCALE = 0.22360679774997896f * 1.4426950408889634f;

#define MFMA16(a, b, c) __builtin_amdgcn_mfma_f32_32x32x16_bf16(__builtin_bit_cast(bfv8, a), __builtin_bit_cast(bfv8, b), c, 0, 0, 0)
#define MFMA8(a, b, c) __builtin_amdgcn_mfma_f32_32x32x8bf16_1k(__builtin_bit_cast(bf16x4, a), __builtin_bit_cast(bf16x4, b), c, 0, 0, 0)

#define SBAR() __builtin_amdgcn_sched_barrier(0)
#define PIN(x) asm volatile("" : "+v"(x))

__device__ __forceinline__ unsigned cvtpk(float lo, float hi) {
    typedef __bf16 bf2 __attribute__((ext_vector_type(2)));
    bf2 v; v[0] = (__bf16)lo; v[1] = (__bf16)hi;
    return __builtin_bit_cast(unsigned, v);
}
__device__ __forceinline__ float bf2f(unsigned v16) { return __uint_as_float(v16 << 16); }
__device__ __forceinline__ float bfr(float x) { return bf2f(cvtpk(x, 0.f) & 0xffffu); }

__device__ __forceinline__ f32x4 rnd4(const f32x4& v) {
    unsigned a = cvtpk(v[0], v[1]), b = cvtpk(v[2], v[3]);
    asm("" : "+v"(a), "+v"(b));
    return f32x4{__uint_as_float(a << 16), __uint_as_float(a & 0xffff0000u), __uint_as_float(b << 16), __uint_as_float(b & 0xffff0000u)};
}
template <int CTRL, int RM> __device__ __forceinline__ float dpp_add(float v) {
    return v + __uint_as_float(__builtin_amdgcn_update_dpp(0u, __float_as_uint(v), CTRL, RM, 0xf, false));
}
__device__ __forceinline__ void st16(char* ws, char* p, const u32x4& v) {
    __builtin_amdgcn_raw_buffer_store_b128(v, __builtin_amdgcn_make_buffer_rsrc(ws, 0, (int)WS_FLAG, 0x00020000), (int)(p - ws), 0, 16);
}
__device__ __forceinline__ void st8(char* ws, char* p, const u32x2& v) {
    __builtin_amdgcn_raw_buffer_store_b64(v, __builtin_amdgcn_make_buffer_rsrc(ws, 0, (int)WS_FLAG, 0x00020000), (int)(p - ws), 0, 16);
}
#define CODE_LINE(FROM, i) ({ unsigned long long base_; unsigned len_; \
    asm volatile("s_getpc_b64 s[60:61]\n\ts_add_u32 s60, s60, " FROM "@rel32@lo+4\n\ts_addc_u32 s61, s61, " FROM "@rel32@hi+12\n\ts_mov_b32 %1, gm_text_end-" FROM \
                 : "={s[60:61]}"(base_), "=s"(len_) :: "scc"); \
    (const char*)base_ + (min((unsigned)(i) * 128u, len_ > 65536u ? 0u : len_) & ~3u); })
__device__ __forceinline__ void touch(const char* p, unsigned lds_dst) {
    unsigned sv;
    asm volatile("s_mov_b32 %0, m0\n\ts_mov_b32 m0, %2\n\ts_nop 0\n\tglobal_load_lds_dword %1, off\n\ts_mov_b32 m0, %0" : "=&s"(sv) : "v"(p), "s"(lds_dst) : "memory");
}
__device__ __forceinline__ void glds16_40(const void* sbase, unsigned voff, unsigned lds_dst) {
    unsigned sv;
    asm volatile("s_mov_b32 %0, m0\n\ts_mov_b32 m0, %3\n\ts_mov_b32 exec_hi, 0xff\n\tglobal_load_lds_dwordx4 %1, %2\n\ts_mov_b32 exec_hi, -1\n\ts_mov_b32 m0, %0"
                 : "=&s"(sv) : "v"(voff), "s"(sbase), "s"(lds_dst) : "memory");
}
template <int N> __device__ __forceinline__ void wait_bar() {
    asm volatile("s_waitcnt vmcnt(%0) lgkmcnt(0)\n\ts_barrier" :: "n"(N) : "memory");
}
typedef short v4i16_t __attribute__((ext_vector_type(4)));
__device__ __forceinline__ bf16x4 ldtr(lds_cptr p) { return __builtin_amdgcn_ds_read_tr16_b64_v4i16((LAS v4i16_t*)p); }

constexpr int K1_X = 0, K1_W = 16384, K1_E = 36864, K1_SLOT = 39488, K1_NS = 4, K1_T = K1_NS * K1_SLOT, K1_LDS = K1_T + 256;

__device__ __forceinline__ int kpos(int r) { return ((r & 12) << 1) | ((r & 16) >> 2) | (r & 3); }
__device__ __forceinline__ u32x2 pack4(const f32x4& a) { return u32x2{cvtpk(a[0], a[1]), cvtpk(a[2], a[3])}; }
#define QUAD(v, i) f32x4{v[4 * (i)], v[4 * (i) + 1], v[4 * (i) + 2], v[4 * (i) + 3]}
#define MFMA1632(a, b, c) __builtin_amdgcn_mfma_f32_16x16x32_bf16(__builtin_bit_cast(bfv8, a), __builtin_bit_cast(bfv8, b), c, 0, 0, 0)

template <int kind  >
__device__ __forceinline__ void k1_mul(char* lds, int mt, int head0, char* __restrict__ ws, int wv, int lane) {
    constexpr bool VOR = (kind == 2);
    const int r16 = lane & 15, g = lane >> 4;
    f32x4 acc[2][10];
#pragma unroll
    for (int t = 0; t < 10; ++t) { acc[0][t] = f32x4{}; acc[1][t] = f32x4{}; }
    lds_cptr xb[2][2];
#pragma unroll
    for (int rt = 0; rt < 2; ++rt) {
        const int row = (kind == 1) ? ((r16 & 4) << 2) + 8 * rt + ((r16 & 8) >> 1) + (r16 & 3) : 16 * rt + r16;
#pragma unroll
        for (int c = 0; c < 2; ++c) xb[c][rt] = (lds_cptr)lds + K1_X + wv * 4096 + row * 128 + 16 * ((4 * c + g) ^ ((row >> 1) & 7));
    }
    const lds_cptr wr = (lds_cptr)lds + K1_W + (8 * g + (r16 >> 2)) * 320 + ((lane >> 1) & 1) * 160 + (lane & 1) * 8;
    lds_cptr wbe = wr + (g & 1) * 16, wbo = wr - (g & 1) * 16;
    bf16x8 xf[2][2], wf[2][10];
    xf[1][0] = bf16x8{}; xf[1][1] = bf16x8{};
#pragma unroll
    for (int t = 0; t < 10; ++t) wf[1][t] = bf16x8{};
    int so = 0;
    gu32* fz = (gu32*)(ws + WS_FLAG) + blockIdx.x * 32 + lane;
#pragma unroll
    for (int t = 0; t < 10; ++t) { PIN(acc[0][t]); PIN(acc[1][t]); if (t >= 4) PIN(wf[1][t]); }
    PIN(xf[1][0]); PIN(xf[1][1]); PIN(xb[0][0]); PIN(xb[0][1]); PIN(xb[1][0]); PIN(xb[1][1]); PIN(wbe); PIN(wbo);
    SBAR();
    asm volatile("s_waitcnt vmcnt(0)" : "+v"(fz) :: "memory");
    if (wv == 1 && lane < 8 && blockIdx.x < 32) __hip_atomic_store(fz, 0u, __ATOMIC_RELAXED, __HIP_MEMORY_SCOPE_AGENT);
#define K1_MM(p, t) do { acc[0][t] = VOR ? MFMA1632(xf[p][0], wf[p][t], acc[0][t]) : MFMA1632(wf[p][t], xf[p][0], acc[0][t]); \
                         acc[1][t] = VOR ? MFMA1632(xf[p][1], wf[p][t], acc[1][t]) : MFMA1632(wf[p][t], xf[p][1], acc[1][t]); } while (0)
#define K1_RD(c, t) do { const lds_cptr q_ = (((t) & 1) ? wbo : wbe) + so + (c) * 10240 + (t) * 16; const bf16x4 lo = ldtr(q_), hi = ldtr(q_ + 1280); \
                         wf[c][t] = __builtin_shufflevector(lo, hi, 0, 1, 2, 3, 4, 5, 6, 7); } while (0)
#define K1_RX(c) do { xf[c][0] = *(LAS const bf16x8*)(xb[c][0] + so); xf[c][1] = *(LAS const bf16x8*)(xb[c][1] + so); } while (0)
#pragma unroll 1
    for (int s = 0; s < 10; ++s) {
        __builtin_amdgcn_s_waitcnt(0xC07F); __builtin_amdgcn_s_barrier(); asm volatile("" ::: "memory"); SBAR();
        K1_RX(0); K1_RD(0, 0); K1_RD(0, 1); SBAR(); K1_MM(1, 4); SBAR();
        K1_RD(0, 2); K1_RD(0, 3); SBAR(); K1_MM(1, 5); SBAR();
        K1_RD(0, 4); K1_RD(0, 5); SBAR(); K1_MM(1, 6); SBAR();
        K1_RD(0, 6); K1_RD(0, 7); SBAR(); K1_MM(1, 7); SBAR();
        K1_RD(0, 8); SBAR(); K1_MM(1, 8); SBAR();
        K1_RD(0, 9); SBAR(); K1_MM(1, 9); SBAR();
        K1_RX(1);
#pragma unroll
        for (int t = 0; t < 10; ++t) { K1_RD(1, t); SBAR(); K1_MM(0, t); SBAR(); }
        K1_MM(1, 0); SBAR(); K1_MM(1, 1); SBAR(); K1_MM(1, 2); SBAR(); K1_MM(1, 3); SBAR();
        so = (so == (K1_NS - 1) * K1_SLOT) ? 0 : so + K1_SLOT;
    }
#pragma unroll
    for (int t = 4; t < 10; ++t) K1_MM(1, t);
    if (kind != 0) { SBAR(); __builtin_amdgcn_s_barrier(); SBAR(); }
    const int tile = 4 * mt + wv;
    if (!VOR) {
        const int hh = g & 1;
        const size_t hstride = (kind == 0) ? (size_t)64 * QT_B : (size_t)64 * KV_B;
        char* base = ws + ((kind == 0) ? WS_Q + (size_t)tile * QT_B : WS_KV + (size_t)tile * KV_B) + (size_t)(head0 + 4 * (g >> 1)) * hstride;
#pragma unroll
        for (int rt = 0; rt < 2; ++rt) {
            const int pos = 16 * rt + r16;
#pragma unroll
            for (int m = 0; m < 2; ++m) {
                char* be = base + (size_t)(2 * m) * hstride; char* bo = be + hstride;
                u32x2 q[5];
#pragma unroll
                for (int i = 0; i < 5; ++i) q[i] = pack4(kind == 0 ? acc[rt][5 * m + i] * QSCALE : acc[rt][5 * m + i]);
                st16(ws, be + hh * 512 + pos * 16, u32x4{q[0][0], q[0][1], q[1][0], q[1][1]});
                const u32x2 x0 = hh ? q[2] : q[3], x1 = hh ? q[3] : q[4];
                st16(ws, bo + (1 - hh) * 512 + pos * 16, u32x4{x0[0], x0[1], x1[0], x1[1]});
                st8(ws, (hh ? bo : be) + 1024 + pos * 8, hh ? q[4] : q[2]);
            }
        }
    } else {
#pragma unroll
        for (int t = 0; t < 10; ++t) {
            const int n = 8 * t + (r16 & 7), hl = n / 20, d = n - hl * 20;
            const u32x2 lo = pack4(acc[0][t]), hi = pack4(acc[1][t]);
            st16(ws, ws + WS_KV + ((size_t)(head0 + 4 * (r16 >> 3) + hl) * 64 + tile) * KV_B + VOFF(d, g), u32x4{lo[0], lo[1], hi[0], hi[1]});
        }
    }

}

struct K1Stream { unsigned xof[4], wof[5], eof; const u16* x; const u16* wsrc; const u16* esrc; int ldw; unsigned lds0; int wv; };
#define NEXTK(k) ((k) == 9 ? 0 : (k) + 1)
#define NEXTS(o) ((o) == (K1_NS - 1) * K1_SLOT ? 0 : (o) + K1_SLOT)
#define NXT "s_add_u32 m0, m0, 0x1000\n\ts_nop 0\n\t"
__device__ __forceinline__ void k1_issue_x(const K1Stream& t, int slot_off, int sk) {
    const unsigned dst = t.lds0 + slot_off + K1_X + 1024 * t.wv; const char* bx = (const char*)t.x + sk * 128; unsigned sv;
    asm volatile("s_mov_b32 %0, m0\n\ts_mov_b32 m0, %1\n\ts_nop 0\n\t"
                 "global_load_lds_dwordx4 %3, %2\n\t" NXT "global_load_lds_dwordx4 %4, %2\n\t" NXT "global_load_lds_dwordx4 %5, %2\n\t" NXT "global_load_lds_dwordx4 %6, %2\n\ts_mov_b32 m0, %0"
                 : "=&s"(sv) : "s"(dst), "s"(bx), "v"(t.xof[0]), "v"(t.xof[1]), "v"(t.xof[2]), "v"(t.xof[3]) : "scc", "memory");
}
template <bool EXTRA>
__device__ __forceinline__ void k1_issue_w(const K1Stream& t, int slot_off, int sk) {
    const unsigned dst = t.lds0 + slot_off + K1_W + 1024 * t.wv; const char* bw = (const char*)t.wsrc + (size_t)sk * 64 * t.ldw * 2; unsigned sv;
    asm volatile("s_mov_b32 %0, m0\n\ts_mov_b32 m0, %1\n\ts_nop 0\n\t"
                 "global_load_lds_dwordx4 %3, %2\n\t" NXT "global_load_lds_dwordx4 %4, %2\n\t" NXT "global_load_lds_dwordx4 %5, %2\n\t" NXT "global_load_lds_dwordx4 %6, %2\n\t" NXT
                 "global_load_lds_dwordx4 %7, %2\n\ts_mov_b32 m0, %0"
                 : "=&s"(sv) : "s"(dst), "s"(bw), "v"(t.wof[0]), "v"(t.wof[1]), "v"(t.wof[2]), "v"(t.wof[3]), "v"(t.wof[4]) : "scc", "memory");
    if (EXTRA) glds16_40((const char*)t.esrc + sk * 2560, t.eof, t.lds0 + slot_off + K1_E + t.wv * 640);
}
template <bool EXTRA> __device__ __forceinline__ void k1_issue(const K1Stream& t, int slot_off, int sk) { k1_issue_x(t, slot_off, sk); k1_issue_w<EXTRA>(t, slot_off, sk); }

template <int EXTRA  >
__device__ __forceinline__ void k1_load(char* lds, const K1Stream& st, const u16* __restrict__ gw, const u16* __restrict__ woA, const u16* __restrict__ woB,
                                        int b, int mt, int type, char* __restrict__ ws, int wv, int lane, int rot) {
    constexpr int CNT = EXTRA ? 10 : 9;
    __builtin_amdgcn_s_setprio(3);
    const int r32 = lane & 31, hh = lane >> 5;
    const int grow = 128 * mt + ((16 * type + 4 * wv + (lane >> 4)) & 127);
    u32x4 ga[5], gc[5];
    f32x16 acc = f32x16{};
    const lds_cptr xb = (lds_cptr)lds + K1_X + (type & 3) * 4096 + r32 * 128 + 16 * ((2 * wv + hh) ^ ((r32 >> 1) & 7));
    const lds_cptr eb = (lds_cptr)lds + K1_E + (16 * wv + 8 * hh + ((lane & 15) >> 2)) * 40 + ((lane >> 4) & 1) * 32 + (lane & 3) * 8;
    const int it0 = min((((b >> 3) * 4 + (b & 3)) * 4 + wv) * 2, 798);
    unsigned r[16];
    int so = 0, so2 = 2 * K1_SLOT, sk2 = NEXTK(NEXTK(rot));
    auto step = [&](int more) {
        bf16x8 xf, wf;
        if (EXTRA) {
            xf = *(LAS const bf16x8*)(xb + so);
            const bf16x4 lo = ldtr(eb + so), hi = ldtr(eb + so + 160);
            wf = __builtin_shufflevector(lo, hi, 0, 1, 2, 3, 4, 5, 6, 7);
        }
        SBAR();
        if (more) k1_issue<EXTRA != 0>(st, so2, sk2);
        SBAR();
        if (EXTRA) acc = (EXTRA == 2) ? MFMA16(xf, wf, acc) : MFMA16(wf, xf, acc);
        SBAR();
        so = NEXTS(so); so2 = NEXTS(so2); sk2 = NEXTK(sk2);
    };
#pragma unroll 1
    for (int s = 0; s < 8; ++s) { wait_bar<CNT>(); step(1); }
    wait_bar<CNT>(); step(0);
    if (!EXTRA) {
#pragma unroll
        for (int u = 0; u < 2; ++u) {
            const int P = (it0 + u) / 5, m = P / 20, i = P - m * 20;
            const unsigned co = ((it0 + u - P * 5) * 64 + lane) * 4;
#pragma unroll
            for (int e = 0; e < 8; ++e) {
                const int hd = 8 * m + ((i < 16) ? (i >> 1) : 2 * (i - 16) + (e >> 2));
                const int d = (i < 16) ? (e & 3) + 8 * (e >> 2) + 4 * (i & 1) : 16 + (e & 3);
                const u16* src = (hd < 32) ? woA + (size_t)(hd * 20 + d) * DM : woB + (size_t)((hd - 32) * 20 + d) * DM;
                asm volatile("global_load_dword %0, %1, %2" : "=&v"(r[8 * u + e]) : "v"(co), "s"(src) : "memory");
            }
        }
    }
    wait_bar<(EXTRA ? 0 : 16)>(); step(0);
    if (!EXTRA) {
        const unsigned go = (lane & 15) * 16, xo = grow * (DM * 2) + go;
#pragma unroll
        for (int i = 0; i < 5; ++i) asm volatile("global_load_dwordx4 %0, %2, %4 offset:%6\n\tglobal_load_dwordx4 %1, %3, %5 offset:%6"
                                                 : "=&v"(ga[i]), "=&v"(gc[i]) : "v"(xo), "v"(go), "s"(st.x), "s"(gw), "n"(i * 256) : "memory");
    }
    char* red = lds + 2 * K1_SLOT + lane * 16;
    if (EXTRA && wv) {
#pragma unroll
        for (int i = 0; i < 4; ++i) *(f32x4*)(red + (wv * 4 + i) * 1024) = f32x4{acc[4 * i], acc[4 * i + 1], acc[4 * i + 2], acc[4 * i + 3]};
    }
    if (EXTRA) asm volatile("s_waitcnt lgkmcnt(0)\n\ts_barrier" ::: "memory");
    if (!EXTRA) {
        __builtin_amdgcn_s_setprio(0);
        asm volatile("s_waitcnt vmcnt(0)" : "+v"(r[0]), "+v"(r[1]), "+v"(r[2]), "+v"(r[3]), "+v"(r[4]), "+v"(r[5]), "+v"(r[6]), "+v"(r[7]),
                     "+v"(r[8]), "+v"(r[9]), "+v"(r[10]), "+v"(r[11]), "+v"(r[12]), "+v"(r[13]), "+v"(r[14]), "+v"(r[15]) :: "memory");
        if (b < 200) {
#pragma unroll
            for (int u = 0; u < 2; ++u) {
                u32x4 c0, c1;
#pragma unroll
                for (int e = 0; e < 4; ++e) {
                    const unsigned lo = r[8 * u + 2 * e], hi = r[8 * u + 2 * e + 1];
                    auto sw = __builtin_amdgcn_permlane32_swap(__builtin_amdgcn_perm(hi, lo, 0x05040100u), __builtin_amdgcn_perm(hi, lo, 0x07060302u), false, false);
                    c0[e] = sw[0]; c1[e] = sw[1];
                }
                const int P = (it0 + u) / 5, n0 = (it0 + u - P * 5) * 128 + 2 * r32 + hh;
                char* dst = ws + WS_WO + (size_t)(P >> 2) * 1024 + (P & 3) * 256;
                st16(ws, dst + (size_t)(n0 >> 4) * 40960 + (n0 & 15) * 16, c0);
                st16(ws, dst + (size_t)((n0 + 64) >> 4) * 40960 + (n0 & 15) * 16, c1);
            }
        }
        float gsum = 0.f;
#pragma unroll
        for (int i = 0; i < 5; ++i) {
            asm volatile("" : "+v"(ga[i]), "+v"(gc[i]));
#pragma unroll
            for (int e = 0; e < 4; ++e) {
                unsigned pr = cvtpk(bf2f(ga[i][e] & 0xffffu) * bf2f(gc[i][e] & 0xffffu), __uint_as_float(ga[i][e] & 0xffff0000u) * __uint_as_float(gc[i][e] & 0xffff0000u));
                asm("" : "+v"(pr));
                gsum += bf2f(pr & 0xffffu) + __uint_as_float(pr & 0xffff0000u);
            }
        }
        gsum = dpp_add<0x111, 0xf>(gsum); gsum = dpp_add<0x112, 0xf>(gsum); gsum = dpp_add<0x114, 0xf>(gsum); gsum = dpp_add<0x118, 0xf>(gsum);
        const float g = bfr(__builtin_amdgcn_rcpf(1.0f + __expf(-bfr(gsum))));
        if ((lane & 15) == 15) __hip_atomic_store((gu32*)(ws + WS_G) + grow, __float_as_uint(g), __ATOMIC_RELAXED, __HIP_MEMORY_SCOPE_AGENT);
    }
    if (EXTRA) {
        if (wv == 0) {
#pragma unroll
            for (int k = 1; k < 4; ++k)
#pragma unroll
                for (int i = 0; i < 4; ++i) { const f32x4 v = *(const f32x4*)(red + (k * 4 + i) * 1024); acc[4 * i] += v[0]; acc[4 * i + 1] += v[1]; acc[4 * i + 2] += v[2]; acc[4 * i + 3] += v[3]; }
            char* be = ws + WS_KV + (size_t)(32 * 64 + 4 * mt + (type & 3)) * KV_B;
            if (EXTRA == 1) {
                const u32x2 q0 = pack4(QUAD(acc, 0)), q1 = pack4(QUAD(acc, 1)); const int pos = kpos(r32);
                st16(ws, be + hh * 512 + pos * 16, u32x4{q0[0], q0[1], q1[0], q1[1]});
                if (hh == 0) st8(ws, be + 1024 + pos * 8, pack4(QUAD(acc, 2)));
            } else if (r32 < 20) {
                char* d = be + VOFF(r32, hh);
                const u32x2 q0 = pack4(QUAD(acc, 0)), q1 = pack4(QUAD(acc, 1)), q2 = pack4(QUAD(acc, 2)), q3 = pack4(QUAD(acc, 3));
                st16(ws, d, u32x4{q0[0], q0[1], q2[0], q2[1]}); st16(ws, d + 64, u32x4{q1[0], q1[1], q3[0], q3[1]});
            }
        }
    }

}

extern "C" __global__ __launch_bounds__(512, 2) void gm_k1_proj(const u16* __restrict__ x, const u16* __restrict__ wqA, const u16* __restrict__ wkA, const u16* __restrict__ wvA,
                                                  const u16* __restrict__ wqkv, const u16* __restrict__ gw, const u16* __restrict__ woA,
                                                  const u16* __restrict__ woB, char* __restrict__ ws) {
    __shared__ __attribute__((aligned(16))) char lds[K1_LDS];
    SBAR();
    const int b = blockIdx.x, xcd = b & 7, loc = b >> 3;
    const int mt = (xcd & 1) * 8 + (loc & 7), type = (xcd >> 1) * 4 + (loc >> 3);
    const int cls = type >> 2, j = type & 3;
    const int lane = threadIdx.x & 63, w = __builtin_amdgcn_readfirstlane(threadIdx.x >> 6), wv = w & 3;
    K1Stream st;
    st.ldw = (cls == 0) ? 640 : 1920;
    st.lds0 = (unsigned)(uintptr_t)lds; st.wv = wv; st.eof = wv * 640 + lane * 16;
#pragma unroll
    for (int i = 0; i < 4; ++i) { const int row = 8 * (wv + 4 * i) + (lane >> 3); st.xof[i] = (unsigned)((128 * mt + row) * (DM * 2) + 16 * ((lane & 7) ^ ((row >> 1) & 7))); }
#pragma unroll
    for (int i = 0; i < 5; ++i) { const int id = 64 * (wv + 4 * i) + lane; const int row = id / 20, c = id - row * 20; st.wof[i] = (unsigned)(row * st.ldw * 2 + 16 * (c ^ ((row >> 3) & 1))); }
    asm volatile("" : "+v"(st.xof[0]), "+v"(st.wof[0]), "+v"(st.wof[1]), "+v"(st.wof[2]), "+v"(st.wof[3]), "+v"(st.wof[4]));
    SBAR();
    st.x = x; st.wsrc = ((cls == 0) ? wqA : wqkv + (cls - 1) * 640) + 160 * j; st.esrc = (cls == 3) ? wvA : wkA;
    const int rot = ((loc ^ (loc >> 3)) & 1) * 5;
    k1_issue<true>(st, w < 4 ? 0 : K1_SLOT, w < 4 ? rot : NEXTK(rot));
    if (w == 7) touch(CODE_LINE("gm_k1_proj", (loc & 3) * 64 + lane), st.lds0 + K1_T);
    if (w >= 4) {
        if (cls == 2) k1_load<1>(lds, st, gw, woA, woB, b, mt, type, ws, wv, lane, rot);
        else if (cls == 3) k1_load<2>(lds, st, gw, woA, woB, b, mt, type, ws, wv, lane, rot);
        else k1_load<0>(lds, st, gw, woA, woB, b, mt, type, ws, wv, lane, rot);
    } else {
        const int head0 = 8 * j + ((cls == 1) ? 32 : 0);
        if (cls == 3) k1_mul<2>(lds, mt, head0, ws, wv, lane);
        else if (cls == 2) k1_mul<1>(lds, mt, head0, ws, wv, lane);
        else k1_mul<0>(lds, mt, head0, ws, wv, lane);
    }
}

constexpr int K2_LDS = 64 * KV_B;
#define K2_LOOP_ASM \
    "global_load_dwordx4 v[84:87], v114, %[qg]\n\tglobal_load_dwordx4 v[80:83], v114, %[qg] offset:1280\n\tglobal_load_dword v90, v115, %[qg]\n\tglobal_load_dword v88, v115, %[qg] offset:1280\n\t" \
    "s_mov_b32 s43, m0\n\ts_mov_b32 m0, %[ldst]\n\ts_nop 0\n\tglobal_load_lds_dwordx4 v114, s[40:41] offset:0\n\t" \
    "global_load_lds_dwordx4 v114, s[40:41] offset:1024\n\tglobal_load_lds_dwordx4 v114, s[40:41] offset:2048\n\tglobal_load_lds_dwordx4 v114, s[40:41] offset:3072\n\tv_mov_b32 v16, 0\n\t" \
    "v_mov_b32 v17, 0\n\tv_mov_b32 v18, 0\n\tv_mov_b32 v19, 0\n\tv_mov_b32 v20, 0\n\t" \
    "v_mov_b32 v21, 0\n\tv_mov_b32 v22, 0\n\tv_mov_b32 v23, 0\n\tv_mov_b32 v24, 0\n\t" \
    "v_mov_b32 v25, 0\n\tv_mov_b32 v26, 0\n\tv_mov_b32 v27, 0\n\tv_mov_b32 v28, 0\n\t" \
    "v_mov_b32 v29, 0\n\tv_mov_b32 v30, 0\n\tv_mov_b32 v31, 0\n\tv_mov_b32 v32, 0\n\t" \
    "v_mov_b32 v33, 0\n\tv_mov_b32 v34, 0\n\tv_mov_b32 v35, 0\n\tv_mov_b32 v36, 0\n\t" \
    "v_mov_b32 v37, 0\n\tv_mov_b32 v38, 0\n\tv_mov_b32 v39, 0\n\tv_mov_b32 v40, 0\n\t" \
    "v_mov_b32 v41, 0\n\tv_mov_b32 v42, 0\n\tv_mov_b32 v43, 0\n\tv_mov_b32 v44, 0\n\t" \
    "v_mov_b32 v45, 0\n\tv_mov_b32 v46, 0\n\tv_mov_b32 v47, 0\n\tv_mov_b32 v72, 0\n\t" \
    "v_mov_b32 v73, 0\n\tv_mov_b32 v74, 0\n\tv_mov_b32 v75, 0\n\tv_mov_b32 v76, 0\n\t" \
    "v_mov_b32 v77, 0\n\tv_mov_b32 v78, 0\n\tv_mov_b32 v79, 0\n\tv_mov_b32 v101, v100\n\t" \
    "v_mov_b32 v102, v100\n\tv_mov_b32 v103, v100\n\tv_mov_b32 v104, v100\n\tv_mov_b32 v105, v100\n\t" \
    "v_mov_b32 v106, v100\n\tv_mov_b32 v107, v100\n\tv_mov_b32 v108, v100\n\tv_mov_b32 v109, v100\n\t" \
    "v_mov_b32 v110, v100\n\tv_mov_b32 v111, v100\n\tv_mov_b32 v97, 0x3f80\n\tv_mov_b32 v98, 0xdddddddd\n\t" \
    "v_mov_b32 v91, 0\n\tv_mov_b32 v89, 0\n\ts_waitcnt vmcnt(0)\n\ts_barrier\n\t" \
    "ds_read_b128 v[92:95], v112\n\tds_read_b32 v96, v113\n\ts_mov_b64 exec, %[vmask]\n\tds_read_b128 v[100:103], v99\n\t" \
    "ds_read_b128 v[104:107], v99 offset:64\n\ts_mov_b64 exec, -1\n\ts_waitcnt lgkmcnt(2)\n\tv_mfma_f32_32x32x16_bf16 v[128:143], v[92:95], v[84:87], 0\n\t" \
    "v_mfma_f32_32x32x16_bf16 v[144:159], v[92:95], v[80:83], 0\n\tv_mfma_f32_32x32x8_bf16 v[128:143], v[96:97], v[90:91], v[128:143]\n\tv_mfma_f32_32x32x8_bf16 v[144:159], v[96:97], v[88:89], v[144:159]\n\tv_mfma_f32_32x32x16_bf16 v[48:63], v[92:95], v[84:87], 0\n\t" \
    "s_nop 10\n\tv_max3_f32 v128, v128, v129, v130\n\tv_max3_f32 v131, v131, v132, v133\n\tv_max3_f32 v134, v134, v135, v136\n\t" \
    "v_max3_f32 v137, v137, v138, v139\n\tv_max3_f32 v140, v140, v141, v142\n\tv_max3_f32 v128, v128, v131, v134\n\tv_max3_f32 v137, v137, v140, v143\n\t" \
    "v_max_f32 v128, v128, v137\n\tv_mov_b32 v129, v128\n\tv_max3_f32 v144, v144, v145, v146\n\tv_max3_f32 v147, v147, v148, v149\n\t" \
    "v_max3_f32 v150, v150, v151, v152\n\tv_max3_f32 v153, v153, v154, v155\n\tv_max3_f32 v156, v156, v157, v158\n\tv_max3_f32 v144, v144, v147, v150\n\t" \
    "v_max3_f32 v153, v153, v156, v159\n\tv_max_f32 v144, v144, v153\n\tv_mov_b32 v145, v144\n\ts_nop 1\n\t" \
    "v_permlane32_swap_b32 v128, v129\n\tv_permlane32_swap_b32 v144, v145\n\ts_nop 1\n\tv_max_f32 v128, v128, v129\n\t" \
    "v_max_f32 v144, v144, v145\n\ts_mov_b32 exec_hi, 0\n\tv_cvt_pk_bf16_f32 v91, -v128, 0\n\tv_cvt_pk_bf16_f32 v89, -v144, 0\n\t" \
    "s_mov_b32 exec_hi, -1\n\ts_nop 1\n\tv_mfma_f32_32x32x8_bf16 v[48:63], v[96:97], v[90:91], v[48:63]\n\ts_setprio 1\n\t" \
    "s_mov_b32 s42, 2\n\ts_nop 9\n\t.Lk2a_%=:\n\tv_exp_f32 v48, v48\n\t" \
    "v_exp_f32 v49, v49\n\tv_smfmac_f32_32x32x32_bf16 v[32:47], v[108:111], v[72:79], v98\n\ts_add_u32 s40, s40, 0x8000\n\ts_addc_u32 s41, s41, 0\n\t" \
    "s_add_u32 m0, m0, 0x8000\n\tv_exp_f32 v50, v50\n\tv_exp_f32 v51, v51\n\tv_exp_f32 v52, v52\n\t" \
    "v_mfma_f32_32x32x16_bf16 v[64:79], v[92:95], v[80:83], 0\n\tglobal_load_lds_dwordx4 v114, s[40:41] offset:0\n\tv_exp_f32 v53, v53\n\tv_exp_f32 v54, v54\n\t" \
    "v_exp_f32 v55, v55\n\tv_mfma_f32_32x32x8_bf16 v[64:79], v[96:97], v[88:89], v[64:79]\n\tds_read_b128 v[92:95], v112 offset:2560\n\tds_read_b32 v96, v113 offset:2560\n\t" \
    "v_exp_f32 v56, v56\n\tv_exp_f32 v57, v57\n\tv_exp_f32 v58, v58\n\tv_exp_f32 v59, v59\n\t" \
    "v_exp_f32 v60, v60\n\ts_waitcnt lgkmcnt(2)\n\tv_smfmac_f32_32x32x32_bf16 v[16:31], v[100:103], v[48:55], v98\n\tv_exp_f32 v61, v61\n\t" \
    "v_exp_f32 v62, v62\n\tv_exp_f32 v63, v63\n\tv_exp_f32 v64, v64\n\tv_exp_f32 v65, v65\n\t" \
    "v_smfmac_f32_32x32x32_bf16 v[16:31], v[104:107], v[56:63], v98\n\tv_exp_f32 v66, v66\n\tv_exp_f32 v67, v67\n\tv_exp_f32 v68, v68\n\t" \
    "s_waitcnt lgkmcnt(0)\n\tv_mfma_f32_32x32x16_bf16 v[48:63], v[92:95], v[84:87], 0\n\tglobal_load_lds_dwordx4 v114, s[40:41] offset:1024\n\tv_exp_f32 v69, v69\n\t" \
    "v_exp_f32 v70, v70\n\tv_exp_f32 v71, v71\n\tv_mfma_f32_32x32x8_bf16 v[48:63], v[96:97], v[90:91], v[48:63]\n\tv_exp_f32 v72, v72\n\t" \
    "v_exp_f32 v73, v73\n\tv_exp_f32 v74, v74\n\tv_exp_f32 v75, v75\n\tv_exp_f32 v76, v76\n\t" \
    "v_smfmac_f32_32x32x32_bf16 v[32:47], v[100:103], v[64:71], v98\n\ts_mov_b64 exec, %[vmask]\n\tds_read_b128 v[100:103], v99 offset:2560\n\tds_read_b128 v[108:111], v99 offset:2624\n\t" \
    "s_mov_b64 exec, -1\n\tv_exp_f32 v77, v77\n\tv_exp_f32 v78, v78\n\tv_exp_f32 v79, v79\n\t" \
    "v_exp_f32 v48, v48\n\tv_exp_f32 v49, v49\n\tv_smfmac_f32_32x32x32_bf16 v[32:47], v[104:107], v[72:79], v98\n\tv_exp_f32 v50, v50\n\t" \
    "v_exp_f32 v51, v51\n\tv_exp_f32 v52, v52\n\tv_mfma_f32_32x32x16_bf16 v[64:79], v[92:95], v[80:83], 0\n\tglobal_load_lds_dwordx4 v114, s[40:41] offset:2048\n\t" \
    "v_exp_f32 v53, v53\n\tv_exp_f32 v54, v54\n\tv_exp_f32 v55, v55\n\tv_mfma_f32_32x32x8_bf16 v[64:79], v[96:97], v[88:89], v[64:79]\n\t" \
    "ds_read_b128 v[92:95], v112 offset:5120\n\tds_read_b32 v96, v113 offset:5120\n\tv_exp_f32 v56, v56\n\tv_exp_f32 v57, v57\n\t" \
    "v_exp_f32 v58, v58\n\tv_exp_f32 v59, v59\n\tv_exp_f32 v60, v60\n\ts_waitcnt lgkmcnt(2)\n\t" \
    "v_smfmac_f32_32x32x32_bf16 v[16:31], v[100:103], v[48:55], v98\n\tv_exp_f32 v61, v61\n\tv_exp_f32 v62, v62\n\tv_exp_f32 v63, v63\n\t" \
    "v_exp_f32 v64, v64\n\tv_exp_f32 v65, v65\n\tv_smfmac_f32_32x32x32_bf16 v[16:31], v[108:111], v[56:63], v98\n\tv_exp_f32 v66, v66\n\t" \
    "v_exp_f32 v67, v67\n\tv_exp_f32 v68, v68\n\ts_waitcnt lgkmcnt(0)\n\tv_mfma_f32_32x32x16_bf16 v[48:63], v[92:95], v[84:87], 0\n\t" \
    "global_load_lds_dwordx4 v114, s[40:41] offset:3072\n\tv_exp_f32 v69, v69\n\tv_exp_f32 v70, v70\n\tv_exp_f32 v71, v71\n\t" \
    "v_mfma_f32_32x32x8_bf16 v[48:63], v[96:97], v[90:91], v[48:63]\n\tv_exp_f32 v72, v72\n\tv_exp_f32 v73, v73\n\tv_exp_f32 v74, v74\n\t" \
    "v_exp_f32 v75, v75\n\tv_exp_f32 v76, v76\n\tv_smfmac_f32_32x32x32_bf16 v[32:47], v[100:103], v[64:71], v98\n\ts_mov_b64 exec, %[vmask]\n\t" \
    "ds_read_b128 v[100:103], v99 offset:5120\n\tds_read_b128 v[104:107], v99 offset:5184\n\ts_mov_b64 exec, -1\n\tv_exp_f32 v77, v77\n\t" \
    "v_exp_f32 v78, v78\n\tv_exp_f32 v79, v79\n\tv_exp_f32 v48, v48\n\tv_exp_f32 v49, v49\n\t" \
    "v_smfmac_f32_32x32x32_bf16 v[32:47], v[108:111], v[72:79], v98\n\ts_add_u32 s40, s40, 0x8000\n\ts_addc_u32 s41, s41, 0\n\ts_add_u32 m0, m0, 0x8000\n\t" \
    "v_exp_f32 v50, v50\n\tv_exp_f32 v51, v51\n\tv_exp_f32 v52, v52\n\tv_mfma_f32_32x32x16_bf16 v[64:79], v[92:95], v[80:83], 0\n\t" \
    "global_load_lds_dwordx4 v114, s[40:41] offset:0\n\tv_exp_f32 v53, v53\n\tv_exp_f32 v54, v54\n\tv_exp_f32 v55, v55\n\t" \
    "v_mfma_f32_32x32x8_bf16 v[64:79], v[96:97], v[88:89], v[64:79]\n\tds_read_b128 v[92:95], v112 offset:7680\n\tds_read_b32 v96, v113 offset:7680\n\tv_exp_f32 v56, v56\n\t" \
    "v_exp_f32 v57, v57\n\tv_exp_f32 v58, v58\n\tv_exp_f32 v59, v59\n\tv_exp_f32 v60, v60\n\t" \
    "s_waitcnt lgkmcnt(2)\n\tv_smfmac_f32_32x32x32_bf16 v[16:31], v[100:103], v[48:55], v98\n\tv_exp_f32 v61, v61\n\tv_exp_f32 v62, v62\n\t" \
    "v_exp_f32 v63, v63\n\tv_exp_f32 v64, v64\n\tv_exp_f32 v65, v65\n\tv_smfmac_f32_32x32x32_bf16 v[16:31], v[104:107], v[56:63], v98\n\t" \
    "v_exp_f32 v66, v66\n\tv_exp_f32 v67, v67\n\tv_exp_f32 v68, v68\n\ts_waitcnt lgkmcnt(0)\n\t" \
    "v_mfma_f32_32x32x16_bf16 v[48:63], v[92:95], v[84:87], 0\n\tglobal_load_lds_dwordx4 v114, s[40:41] offset:1024\n\tv_exp_f32 v69, v69\n\tv_exp_f32 v70, v70\n\t" \
    "v_exp_f32 v71, v71\n\tv_mfma_f32_32x32x8_bf16 v[48:63], v[96:97], v[90:91], v[48:63]\n\tv_exp_f32 v72, v72\n\tv_exp_f32 v73, v73\n\t" \
    "v_exp_f32 v74, v74\n\tv_exp_f32 v75, v75\n\tv_exp_f32 v76, v76\n\tv_smfmac_f32_32x32x32_bf16 v[32:47], v[100:103], v[64:71], v98\n\t" \
    "s_mov_b64 exec, %[vmask]\n\tds_read_b128 v[100:103], v99 offset:7680\n\tds_read_b128 v[108:111], v99 offset:7744\n\ts_mov_b64 exec, -1\n\t" \
    "v_exp_f32 v77, v77\n\tv_exp_f32 v78, v78\n\tv_exp_f32 v79, v79\n\tv_exp_f32 v48, v48\n\t" \
    "v_exp_f32 v49, v49\n\tv_smfmac_f32_32x32x32_bf16 v[32:47], v[104:107], v[72:79], v98\n\tv_exp_f32 v50, v50\n\tv_exp_f32 v51, v51\n\t" \
    "v_exp_f32 v52, v52\n\tv_mfma_f32_32x32x16_bf16 v[64:79], v[92:95], v[80:83], 0\n\tglobal_load_lds_dwordx4 v114, s[40:41] offset:2048\n\tv_exp_f32 v53, v53\n\t" \
    "v_exp_f32 v54, v54\n\tv_exp_f32 v55, v55\n\tv_mfma_f32_32x32x8_bf16 v[64:79], v[96:97], v[88:89], v[64:79]\n\tds_read_b128 v[92:95], v112 offset:10240\n\t" \
    "ds_read_b32 v96, v113 offset:10240\n\tv_exp_f32 v56, v56\n\tv_exp_f32 v57, v57\n\tv_exp_f32 v58, v58\n\t" \
    "v_exp_f32 v59, v59\n\tv_exp_f32 v60, v60\n\ts_waitcnt lgkmcnt(2)\n\tv_smfmac_f32_32x32x32_bf16 v[16:31], v[100:103], v[48:55], v98\n\t" \
    "v_exp_f32 v61, v61\n\tv_exp_f32 v62, v62\n\tv_exp_f32 v63, v63\n\tv_exp_f32 v64, v64\n\t" \
    "v_exp_f32 v65, v65\n\tv_smfmac_f32_32x32x32_bf16 v[16:31], v[108:111], v[56:63], v98\n\tv_exp_f32 v66, v66\n\tv_exp_f32 v67, v67\n\t" \
    "v_exp_f32 v68, v68\n\ts_waitcnt lgkmcnt(0)\n\tv_mfma_f32_32x32x16_bf16 v[48:63], v[92:95], v[84:87], 0\n\tglobal_load_lds_dwordx4 v114, s[40:41] offset:3072\n\t" \
    "v_exp_f32 v69, v69\n\tv_exp_f32 v70, v70\n\tv_exp_f32 v71, v71\n\tv_mfma_f32_32x32x8_bf16 v[48:63], v[96:97], v[90:91], v[48:63]\n\t" \
    "v_exp_f32 v72, v72\n\tv_exp_f32 v73, v73\n\tv_exp_f32 v74, v74\n\tv_exp_f32 v75, v75\n\t" \
    "v_exp_f32 v76, v76\n\tv_smfmac_f32_32x32x32_bf16 v[32:47], v[100:103], v[64:71], v98\n\ts_mov_b64 exec, %[vmask]\n\tds_read_b128 v[100:103], v99 offset:10240\n\t" \
    "ds_read_b128 v[104:107], v99 offset:10304\n\ts_mov_b64 exec, -1\n\tv_exp_f32 v77, v77\n\tv_exp_f32 v78, v78\n\t" \
    "v_exp_f32 v79, v79\n\tv_add_u32 v99, 10240, v99\n\tv_add_u32 v112, 10240, v112\n\tv_add_u32 v113, 10240, v113\n\t" \
    "s_setprio 0\n\ts_sub_u32 s42, s42, 1\n\ts_cmp_lg_u32 s42, 0\n\ts_cbranch_scc1 .Lk2a_%=\n\t" \
    "s_mov_b32 m0, s43\n\ts_waitcnt vmcnt(8)\n\ts_barrier\n\ts_setprio 3\n\t" \
    "s_mov_b32 s42, 3\n\ts_call_b64 s[44:45], .Lk2b_%=\n\ts_setprio 2\n\ts_mov_b32 s42, 2\n\t" \
    "s_call_b64 s[44:45], .Lk2b_%=\n\ts_setprio 1\n\ts_mov_b32 s42, 1\n\ts_call_b64 s[44:45], .Lk2b_%=\n\t" \
    "s_waitcnt vmcnt(0)\n\ts_barrier\n\ts_setprio 3\n\ts_mov_b32 s42, 4\n\t" \
    "s_call_b64 s[44:45], .Lk2b_%=\n\tglobal_load_dwordx4 v[128:131], v114, s[46:47] offset:-1024\n\tglobal_load_dwordx4 v[148:151], v114, s[46:47] offset:0\n\tglobal_load_dwordx4 v[168:171], v114, s[46:47] offset:1024\n\t" \
    "global_load_dwordx4 v[188:191], v114, s[46:47] offset:2048\n\tglobal_load_dwordx4 v[208:211], v114, s[46:47] offset:3072\n\ts_add_u32 s46, s46, 0xa000\n\ts_addc_u32 s47, s47, 0\n\t" \
    "global_load_dwordx4 v[132:135], v114, s[46:47] offset:-1024\n\tglobal_load_dwordx4 v[152:155], v114, s[46:47] offset:0\n\tglobal_load_dwordx4 v[172:175], v114, s[46:47] offset:1024\n\tglobal_load_dwordx4 v[192:195], v114, s[46:47] offset:2048\n\t" \
    "global_load_dwordx4 v[212:215], v114, s[46:47] offset:3072\n\ts_add_u32 s46, s46, 0xa000\n\ts_addc_u32 s47, s47, 0\n\tglobal_load_dwordx4 v[136:139], v114, s[46:47] offset:-1024\n\t" \
    "global_load_dwordx4 v[156:159], v114, s[46:47] offset:0\n\tglobal_load_dwordx4 v[176:179], v114, s[46:47] offset:1024\n\tglobal_load_dwordx4 v[196:199], v114, s[46:47] offset:2048\n\tglobal_load_dwordx4 v[216:219], v114, s[46:47] offset:3072\n\t" \
    "s_add_u32 s46, s46, 0xa000\n\ts_addc_u32 s47, s47, 0\n\tglobal_load_dwordx4 v[140:143], v114, s[46:47] offset:-1024\n\tglobal_load_dwordx4 v[160:163], v114, s[46:47] offset:0\n\t" \
    "global_load_dwordx4 v[180:183], v114, s[46:47] offset:1024\n\tglobal_load_dwordx4 v[200:203], v114, s[46:47] offset:2048\n\tglobal_load_dwordx4 v[220:223], v114, s[46:47] offset:3072\n\ts_add_u32 s46, s46, 0xa000\n\t" \
    "s_addc_u32 s47, s47, 0\n\tglobal_load_dwordx4 v[144:147], v114, s[46:47] offset:-1024\n\tglobal_load_dwordx4 v[164:167], v114, s[46:47] offset:0\n\tglobal_load_dwordx4 v[184:187], v114, s[46:47] offset:1024\n\t" \
    "global_load_dwordx4 v[204:207], v114, s[46:47] offset:2048\n\tglobal_load_dwordx4 v[224:227], v114, s[46:47] offset:3072\n\tglobal_load_dword v228, v116, %[gp]\n\tglobal_load_dword v229, v117, %[gp]\n\t" \
    "global_load_dword v230, v118, %[gp]\n\ts_setprio 2\n\ts_mov_b32 s42, 2\n\ts_call_b64 s[44:45], .Lk2b_%=\n\t" \
    "s_setprio 1\n\ts_mov_b32 s42, 1\n\ts_call_b64 s[44:45], .Lk2b_%=\n\ts_setprio 0\n\t" \
    "s_mov_b32 s42, 1\n\ts_call_b64 s[44:45], .Lk2b_%=\n\ts_branch .Lk2e_%=\n\t.Lk2b_%=:\n\t" \
    "v_exp_f32 v48, v48\n\tv_exp_f32 v49, v49\n\tv_smfmac_f32_32x32x32_bf16 v[32:47], v[108:111], v[72:79], v98\n\tv_exp_f32 v50, v50\n\t" \
    "v_exp_f32 v51, v51\n\tv_exp_f32 v52, v52\n\tv_mfma_f32_32x32x16_bf16 v[64:79], v[92:95], v[80:83], 0\n\tv_exp_f32 v53, v53\n\t" \
    "v_exp_f32 v54, v54\n\tv_exp_f32 v55, v55\n\tv_mfma_f32_32x32x8_bf16 v[64:79], v[96:97], v[88:89], v[64:79]\n\tds_read_b128 v[92:95], v112 offset:2560\n\t" \
    "ds_read_b32 v96, v113 offset:2560\n\tv_exp_f32 v56, v56\n\tv_exp_f32 v57, v57\n\tv_exp_f32 v58, v58\n\t" \
    "v_exp_f32 v59, v59\n\tv_exp_f32 v60, v60\n\ts_waitcnt lgkmcnt(2)\n\tv_smfmac_f32_32x32x32_bf16 v[16:31], v[100:103], v[48:55], v98\n\t" \
    "v_exp_f32 v61, v61\n\tv_exp_f32 v62, v62\n\tv_exp_f32 v63, v63\n\tv_exp_f32 v64, v64\n\t" \
    "v_exp_f32 v65, v65\n\tv_smfmac_f32_32x32x32_bf16 v[16:31], v[104:107], v[56:63], v98\n\tv_exp_f32 v66, v66\n\tv_exp_f32 v67, v67\n\t" \
    "v_exp_f32 v68, v68\n\ts_waitcnt lgkmcnt(0)\n\tv_mfma_f32_32x32x16_bf16 v[48:63], v[92:95], v[84:87], 0\n\tv_exp_f32 v69, v69\n\t" \
    "v_exp_f32 v70, v70\n\tv_exp_f32 v71, v71\n\tv_mfma_f32_32x32x8_bf16 v[48:63], v[96:97], v[90:91], v[48:63]\n\tv_exp_f32 v72, v72\n\t" \
    "v_exp_f32 v73, v73\n\tv_exp_f32 v74, v74\n\tv_exp_f32 v75, v75\n\tv_exp_f32 v76, v76\n\t" \
    "v_smfmac_f32_32x32x32_bf16 v[32:47], v[100:103], v[64:71], v98\n\ts_mov_b64 exec, %[vmask]\n\tds_read_b128 v[100:103], v99 offset:2560\n\tds_read_b128 v[108:111], v99 offset:2624\n\t" \
    "s_mov_b64 exec, -1\n\tv_exp_f32 v77, v77\n\tv_exp_f32 v78, v78\n\tv_exp_f32 v79, v79\n\t" \
    "v_exp_f32 v48, v48\n\tv_exp_f32 v49, v49\n\tv_smfmac_f32_32x32x32_bf16 v[32:47], v[104:107], v[72:79], v98\n\tv_exp_f32 v50, v50\n\t" \
    "v_exp_f32 v51, v51\n\tv_exp_f32 v52, v52\n\tv_mfma_f32_32x32x16_bf16 v[64:79], v[92:95], v[80:83], 0\n\tv_exp_f32 v53, v53\n\t" \
    "v_exp_f32 v54, v54\n\tv_exp_f32 v55, v55\n\tv_mfma_f32_32x32x8_bf16 v[64:79], v[96:97], v[88:89], v[64:79]\n\tds_read_b128 v[92:95], v112 offset:5120\n\t" \
    "ds_read_b32 v96, v113 offset:5120\n\tv_exp_f32 v56, v56\n\tv_exp_f32 v57, v57\n\tv_exp_f32 v58, v58\n\t" \
    "v_exp_f32 v59, v59\n\tv_exp_f32 v60, v60\n\ts_waitcnt lgkmcnt(2)\n\tv_smfmac_f32_32x32x32_bf16 v[16:31], v[100:103], v[48:55], v98\n\t" \
    "v_exp_f32 v61, v61\n\tv_exp_f32 v62, v62\n\tv_exp_f32 v63, v63\n\tv_exp_f32 v64, v64\n\t" \
    "v_exp_f32 v65, v65\n\tv_smfmac_f32_32x32x32_bf16 v[16:31], v[108:111], v[56:63], v98\n\tv_exp_f32 v66, v66\n\tv_exp_f32 v67, v67\n\t" \
    "v_exp_f32 v68, v68\n\ts_waitcnt lgkmcnt(0)\n\tv_mfma_f32_32x32x16_bf16 v[48:63], v[92:95], v[84:87], 0\n\tv_exp_f32 v69, v69\n\t" \
    "v_exp_f32 v70, v70\n\tv_exp_f32 v71, v71\n\tv_mfma_f32_32x32x8_bf16 v[48:63], v[96:97], v[90:91], v[48:63]\n\tv_exp_f32 v72, v72\n\t" \
    "v_exp_f32 v73, v73\n\tv_exp_f32 v74, v74\n\tv_exp_f32 v75, v75\n\tv_exp_f32 v76, v76\n\t" \
    "v_smfmac_f32_32x32x32_bf16 v[32:47], v[100:103], v[64:71], v98\n\ts_mov_b64 exec, %[vmask]\n\tds_read_b128 v[100:103], v99 offset:5120\n\tds_read_b128 v[104:107], v99 offset:5184\n\t" \
    "s_mov_b64 exec, -1\n\tv_exp_f32 v77, v77\n\tv_exp_f32 v78, v78\n\tv_exp_f32 v79, v79\n\t" \
    "v_exp_f32 v48, v48\n\tv_exp_f32 v49, v49\n\tv_smfmac_f32_32x32x32_bf16 v[32:47], v[108:111], v[72:79], v98\n\tv_exp_f32 v50, v50\n\t" \
    "v_exp_f32 v51, v51\n\tv_exp_f32 v52, v52\n\tv_mfma_f32_32x32x16_bf16 v[64:79], v[92:95], v[80:83], 0\n\tv_exp_f32 v53, v53\n\t" \
    "v_exp_f32 v54, v54\n\tv_exp_f32 v55, v55\n\tv_mfma_f32_32x32x8_bf16 v[64:79], v[96:97], v[88:89], v[64:79]\n\tds_read_b128 v[92:95], v112 offset:7680\n\t" \
    "ds_read_b32 v96, v113 offset:7680\n\tv_exp_f32 v56, v56\n\tv_exp_f32 v57, v57\n\tv_exp_f32 v58, v58\n\t" \
    "v_exp_f32 v59, v59\n\tv_exp_f32 v60, v60\n\ts_waitcnt lgkmcnt(2)\n\tv_smfmac_f32_32x32x32_bf16 v[16:31], v[100:103], v[48:55], v98\n\t" \
    "v_exp_f32 v61, v61\n\tv_exp_f32 v62, v62\n\tv_exp_f32 v63, v63\n\tv_exp_f32 v64, v64\n\t" \
    "v_exp_f32 v65, v65\n\tv_smfmac_f32_32x32x32_bf16 v[16:31], v[104:107], v[56:63], v98\n\tv_exp_f32 v66, v66\n\tv_exp_f32 v67, v67\n\t" \
    "v_exp_f32 v68, v68\n\ts_waitcnt lgkmcnt(0)\n\tv_mfma_f32_32x32x16_bf16 v[48:63], v[92:95], v[84:87], 0\n\tv_exp_f32 v69, v69\n\t" \
    "v_exp_f32 v70, v70\n\tv_exp_f32 v71, v71\n\tv_mfma_f32_32x32x8_bf16 v[48:63], v[96:97], v[90:91], v[48:63]\n\tv_exp_f32 v72, v72\n\t" \
    "v_exp_f32 v73, v73\n\tv_exp_f32 v74, v74\n\tv_exp_f32 v75, v75\n\tv_exp_f32 v76, v76\n\t" \
    "v_smfmac_f32_32x32x32_bf16 v[32:47], v[100:103], v[64:71], v98\n\ts_mov_b64 exec, %[vmask]\n\tds_read_b128 v[100:103], v99 offset:7680\n\tds_read_b128 v[108:111], v99 offset:7744\n\t" \
    "s_mov_b64 exec, -1\n\tv_exp_f32 v77, v77\n\tv_exp_f32 v78, v78\n\tv_exp_f32 v79, v79\n\t" \
    "v_exp_f32 v48, v48\n\tv_exp_f32 v49, v49\n\tv_smfmac_f32_32x32x32_bf16 v[32:47], v[104:107], v[72:79], v98\n\tv_exp_f32 v50, v50\n\t" \
    "v_exp_f32 v51, v51\n\tv_exp_f32 v52, v52\n\tv_mfma_f32_32x32x16_bf16 v[64:79], v[92:95], v[80:83], 0\n\tv_exp_f32 v53, v53\n\t" \
    "v_exp_f32 v54, v54\n\tv_exp_f32 v55, v55\n\tv_mfma_f32_32x32x8_bf16 v[64:79], v[96:97], v[88:89], v[64:79]\n\tds_read_b128 v[92:95], v112 offset:10240\n\t" \
    "ds_read_b32 v96, v113 offset:10240\n\tv_exp_f32 v56, v56\n\tv_exp_f32 v57, v57\n\tv_exp_f32 v58, v58\n\t" \
    "v_exp_f32 v59, v59\n\tv_exp_f32 v60, v60\n\ts_waitcnt lgkmcnt(2)\n\tv_smfmac_f32_32x32x32_bf16 v[16:31], v[100:103], v[48:55], v98\n\t" \
    "v_exp_f32 v61, v61\n\tv_exp_f32 v62, v62\n\tv_exp_f32 v63, v63\n\tv_exp_f32 v64, v64\n\t" \
    "v_exp_f32 v65, v65\n\tv_smfmac_f32_32x32x32_bf16 v[16:31], v[108:111], v[56:63], v98\n\tv_exp_f32 v66, v66\n\tv_exp_f32 v67, v67\n\t" \
    "v_exp_f32 v68, v68\n\ts_waitcnt lgkmcnt(0)\n\tv_mfma_f32_32x32x16_bf16 v[48:63], v[92:95], v[84:87], 0\n\tv_exp_f32 v69, v69\n\t" \
    "v_exp_f32 v70, v70\n\tv_exp_f32 v71, v71\n\tv_mfma_f32_32x32x8_bf16 v[48:63], v[96:97], v[90:91], v[48:63]\n\tv_exp_f32 v72, v72\n\t" \
    "v_exp_f32 v73, v73\n\tv_exp_f32 v74, v74\n\tv_exp_f32 v75, v75\n\tv_exp_f32 v76, v76\n\t" \
    "v_smfmac_f32_32x32x32_bf16 v[32:47], v[100:103], v[64:71], v98\n\ts_mov_b64 exec, %[vmask]\n\tds_read_b128 v[100:103], v99 offset:10240\n\tds_read_b128 v[104:107], v99 offset:10304\n\t" \
    "s_mov_b64 exec, -1\n\tv_exp_f32 v77, v77\n\tv_exp_f32 v78, v78\n\tv_exp_f32 v79, v79\n\t" \
    "v_add_u32 v99, 10240, v99\n\tv_add_u32 v112, 10240, v112\n\tv_add_u32 v113, 10240, v113\n\ts_sub_u32 s42, s42, 1\n\t" \
    "s_cmp_lg_u32 s42, 0\n\ts_cbranch_scc1 .Lk2b_%=\n\ts_setpc_b64 s[44:45]\n\t.Lk2e_%=:\n\t" \
    "s_nop 1\n\tv_smfmac_f32_32x32x32_bf16 v[32:47], v[108:111], v[72:79], v98\n\ts_waitcnt vmcnt(0) lgkmcnt(0)\n\ts_nop 15\n\t"
#define K2_CLOBBERS "v48","v49","v50","v51","v52","v53","v54","v55","v56","v57","v58","v59","v60","v61","v62","v63","v64","v65","v66","v67","v68","v69","v70","v71","v72","v73","v74","v75","v76","v77","v78","v79","v89","v91","v92","v93","v94","v95","v96","v97","v98","v101","v102","v103","v104","v105","v106","v107","v108","v109","v110","v111"

__device__ __forceinline__ f32x16 smf(const u32x4& v, const f32x8& p, const f32x16& o, int idx) {
    return __builtin_amdgcn_smfmac_f32_32x32x32_bf16(__builtin_bit_cast(bfv8, v), __builtin_bit_cast(bfv16, p), o, idx, 0, 0);
}
#define LO8(S) __builtin_shufflevector(S, S, 0, 1, 2, 3, 4, 5, 6, 7)
#define HI8(S) __builtin_shufflevector(S, S, 8, 9, 10, 11, 12, 13, 14, 15)

__device__ __forceinline__ float halfmax(float m) {
    auto rr = __builtin_amdgcn_permlane32_swap(__float_as_uint(m), __float_as_uint(m), false, false);
    return fmaxf(__uint_as_float(rr[0]), __uint_as_float(rr[1]));
}
__device__ __forceinline__ float max16(const f32x16& s) {
    float a = fmaxf(fmaxf(s[0], s[1]), s[2]), b = fmaxf(fmaxf(s[3], s[4]), s[5]);
    a = fmaxf(fmaxf(a, s[6]), s[7]); b = fmaxf(fmaxf(b, s[8]), s[9]);
    a = fmaxf(fmaxf(a, s[10]), s[11]); b = fmaxf(fmaxf(b, s[12]), s[13]);
    a = fmaxf(fmaxf(a, s[14]), s[15]);
    return halfmax(fmaxf(a, b));
}

__device__ __forceinline__ f32x16 attn_slow(const char* __restrict__ kvg, int lane, bf16x8 q0, unsigned q1lo, unsigned vc) {
    const int r32 = lane & 31, hh = lane >> 5, vr = r32 < 20 ? r32 : 0;
    const u32x2 q1 = {q1lo, 0u}; const u32x4 vcv = {vc, vc, vc, vc};
    f32x16 o = f32x16{}; float m = -INFINITY;
#pragma unroll 1
    for (int t = 0; t < 64; ++t) {
        const char* b = kvg + (size_t)t * KV_B;
        const bf16x8 k0 = *(const bf16x8*)(b + lane * 16); const u32x2 k1 = {*(const unsigned*)(b + 1024 + r32 * 8 + hh * 4), 0u};
        u32x4 v0 = *(const u32x4*)(b + VOFF(vr, hh)), v1 = *(const u32x4*)(b + VOFF(vr, 2 + hh));
        if (r32 >= 20) { v0 = vcv; v1 = vcv; }
        f32x16 s = MFMA16(k0, q0, f32x16{}); s = MFMA8(k1, q1, s);
        const float mn = fmaxf(m, max16(s)); const float al = __builtin_amdgcn_exp2f(m - mn); m = mn;
#pragma unroll
        for (int i = 0; i < 16; ++i) { o[i] *= al; s[i] = __builtin_amdgcn_exp2f(s[i] - mn); }
        o = smf(v0, LO8(s), o, (int)0xDDDDDDDD); o = smf(v1, HI8(s), o, (int)0xDDDDDDDD);
    }
    return o;
}

__device__ __forceinline__ void store_o(const f32x16& o, __amdgpu_buffer_rsrc_t ors, int hq, int qt, int lane) {
    const int r32 = lane & 31, hh = lane >> 5;
    auto rr = __builtin_amdgcn_permlane32_swap(__float_as_uint(o[8]), __float_as_uint(o[8]), false, false);
    const float inv = __builtin_amdgcn_rcpf(__uint_as_float(rr[1]));
    const int b = ((2 * qt + (r32 >> 4)) * 40 + 5 * (hq >> 3)) * 1024;
    u32x4 v = {cvtpk(o[0] * inv, o[1] * inv), cvtpk(o[2] * inv, o[3] * inv), cvtpk(o[4] * inv, o[5] * inv), cvtpk(o[6] * inv, o[7] * inv)};
    __builtin_amdgcn_raw_buffer_store_b128(v, ors, b + (2 * (hq & 7) + hh) * 256 + (r32 & 15) * 16, 0, 16);
    if (hh == 0) { u32x2 t = {cvtpk(o[8] * inv, o[9] * inv), cvtpk(o[10] * inv, o[11] * inv)}; __builtin_amdgcn_raw_buffer_store_b64(t, ors, b + 4096 + (hq & 7) * 128 + (r32 & 15) * 8, 0, 16); }
}
__device__ __forceinline__ float abs_sum12(const f32x16& o) {
    float a = fabsf(o[0]) + fabsf(o[1]), b = fabsf(o[2]) + fabsf(o[3]);
#pragma unroll
    for (int i = 4; i < 12; i += 2) { a += fabsf(o[i]); b += fabsf(o[i + 1]); }
    return a + b;
}

extern "C" __global__ __launch_bounds__(512, 2) void gm_k2_attn(char* __restrict__ ws, u16* __restrict__ out) {
    __shared__ __attribute__((aligned(16))) char lds[K2_LDS];
    SBAR();
    const int b = blockIdx.x, xcd = b & 7, i5 = b >> 3;
    const int hq = 8 * xcd + (i5 >> 2), qb = i5 & 3, hk = hq < 32 ? 32 : hq - 32;
    const int lane = threadIdx.x & 63, w = __builtin_amdgcn_readfirstlane(threadIdx.x >> 6);
    const int r32 = lane & 31, hh = lane >> 5;
    const unsigned lds0 = (unsigned)(uintptr_t)lds;
    const int qt = 16 * qb + 2 * w;
    const int mt = xcd * 4 + (i5 & 3), nb = i5 >> 2;
    bf16x8 qa0, qb0; unsigned qa1l, qb1l;
    unsigned qo1 = 1024 + r32 * 8 + hh * 4, voff = lane * 16;
    const unsigned ldst = lds0 + w * 4096;
    const int vr = r32 < 20 ? r32 : 0;
    unsigned ak0 = lds0 + lane * 16, ak1 = lds0 + 1024 + r32 * 8 + hh * 4, av = lds0 + VOFF(vr, hh);
    const unsigned vc = (r32 == 20) ? 0x3F803F80u : 0u;
    unsigned v0 = vc;
    f32x16 Oa, Ob;
    float gv[3]; unsigned go[3];
#pragma unroll
    for (int r = 0; r < 3; ++r) go[r] = WS_G + (64 * mt + 16 * (min(w + 8 * r, 19) / 5) + (lane & 15)) * 4;
    unsigned oq = WS_Q + (hq * 64 + qt) * QT_B, ok = WS_KV + hk * 64 * KV_B + w * 4096, ow = WS_WO + (5 * nb * 40 + 5 * w + 1) * 1024;
    asm volatile("" : "+s"(oq), "+s"(ok), "+s"(ow), "+{v99}"(av), "+{v100}"(v0), "+{v112}"(ak0), "+{v113}"(ak1), "+{v114}"(voff), "+{v115}"(qo1), "+{v116}"(go[0]), "+{v117}"(go[1]), "+{v118}"(go[2]));
    SBAR();
    const char* qg = ws + oq; const char* gb = ws + ok;
    f32x16 wr[6]; f32x4 wr6;
    {
        const unsigned long long vmask = 0x000FFFFF000FFFFFull;
        const char* wg = ws + ow;
        asm volatile(K2_LOOP_ASM
                     : "={v[16:31]}"(Oa), "={v[32:47]}"(Ob), "+{v99}"(av), "+{v100}"(v0), "+{v112}"(ak0), "+{v113}"(ak1), "+{s[40:41]}"(gb), "+{s[46:47]}"(wg),
                       "={v[128:143]}"(wr[0]), "={v[144:159]}"(wr[1]), "={v[160:175]}"(wr[2]), "={v[176:191]}"(wr[3]), "={v[192:207]}"(wr[4]), "={v[208:223]}"(wr[5]), "={v[224:227]}"(wr6),
                       "={v[80:83]}"(qb0), "={v[84:87]}"(qa0), "={v88}"(qb1l), "={v90}"(qa1l), "={v228}"(gv[0]), "={v229}"(gv[1]), "={v230}"(gv[2])
                     : "{v114}"(voff), "{v115}"(qo1), "{v116}"(go[0]), "{v117}"(go[1]), "{v118}"(go[2]), [gp] "s"(ws), [qg] "s"(qg), [ldst] "s"(ldst), [vmask] "s"(vmask)
                     : K2_CLOBBERS, "s42", "s43", "s44", "s45", "scc", "memory");
    }
    const __amdgpu_buffer_rsrc_t ors = __builtin_amdgcn_make_buffer_rsrc(ws + WS_O, 0, 128 * 40 * 1024, 0x00020000);
    store_o(Oa, ors, hq, qt, lane);
    store_o(Ob, ors, hq, qt + 1, lane);
    SBAR();
    if (__any(!(abs_sum12(Oa) + abs_sum12(Ob) < 1e36f) || (hh && !(fminf(Oa[8], Ob[8]) > 1e-18f)))) {
        const char* kvg = ws + WS_KV + (size_t)hk * 64 * KV_B;
        asm volatile("s_waitcnt vmcnt(0)" ::: "memory");
        store_o(attn_slow(kvg, lane, qa0, qa1l, vc), ors, hq, qt, lane);
        store_o(attn_slow(kvg, lane, qb0, qb1l, vc), ors, hq, qt + 1, lane);
    }
    gu32* flags = (gu32*)(ws + WS_FLAG);
    gu32* f = flags + ((mt >> 3) * 8 + w) * 32 + (lane & 7);
    gu32* mine = flags + (qb * 8 + (hq >> 3)) * 32 + (hq & 7);
    const __amdgpu_buffer_rsrc_t outr = __builtin_amdgcn_make_buffer_rsrc(out, 0, SEQ * DM * 2, 0x00020000);
    int to = (lane >> 4) * 256 + (lane & 15) * 8; PIN(to);
    float g1[3]; int oo[3];
#pragma unroll
    for (int r = 0; r < 3; ++r) { const int t = w + 8 * r, i = t / 5, j = t - i * 5; g1[r] = bfr(1.0f - gv[r]); oo[r] = ((64 * mt + 16 * i + (lane & 15)) * DM + 80 * nb + 16 * j + 4 * (lane >> 4)) * 2; PIN(g1[r]); PIN(oo[r]); }
    asm volatile("s_waitcnt vmcnt(0)\n\ts_barrier" : "+v"(f), "+s"(mine) :: "memory");
    if (w == (hq >> 3) && lane == 0) __hip_atomic_store(mine, 1u, __ATOMIC_RELAXED, __HIP_MEMORY_SCOPE_AGENT);
    asm volatile("s_waitcnt vmcnt(0)" ::: "memory");
#pragma unroll 1
    for (unsigned spins = 0; spins < (1u << 16); ++spins) {
        if (__all(__hip_atomic_load(f, __ATOMIC_RELAXED, __HIP_MEMORY_SCOPE_AGENT) == 1u)) break;
        __builtin_amdgcn_s_sleep(1);
    }
    __builtin_amdgcn_fence(__ATOMIC_ACQUIRE, "wavefront");
    u32x4 af[4][5];
#define P3_LD(i) do { const int so_ = ((4 * mt + i) * 40 + 5 * w) * 1024; \
        _Pragma("unroll") for (int ks = 0; ks < 4; ++ks) af[i][ks] = __builtin_amdgcn_raw_buffer_load_b128(ors, lane * 16, so_ + ks * 1024, 16); \
        const u32x2 t0_ = __builtin_amdgcn_raw_buffer_load_b64(ors, to, so_ + 4096, 16), t1_ = __builtin_amdgcn_raw_buffer_load_b64(ors, to, so_ + 4096 + 128, 16); \
        af[i][4] = u32x4{t0_[0], t0_[1], t1_[0], t1_[1]}; } while (0)
    P3_LD(0);
    SBAR();
#define WFRAG(n) ((n) < 24 ? __builtin_shufflevector(wr[(n) < 24 ? (n) / 4 : 0], wr[(n) < 24 ? (n) / 4 : 0], ((n) & 3) * 4, ((n) & 3) * 4 + 1, ((n) & 3) * 4 + 2, ((n) & 3) * 4 + 3) : wr6)
#define P3_MM(i, j) do { f32x4 c_ = f32x4{}; \
        c_ = MFMA1632(WFRAG(0 + j), af[i][0], c_); \
        c_ = MFMA1632(WFRAG(5 + j), af[i][1], c_); \
        c_ = MFMA1632(WFRAG(10 + j), af[i][2], c_); \
        c_ = MFMA1632(WFRAG(15 + j), af[i][3], c_); \
        c_ = MFMA1632(WFRAG(20 + j), af[i][4], c_); \
        *(f32x4*)(lds + (w * 20 + (i) * 5 + (j)) * 1024 + lane * 16) = c_; } while (0)
#define P3_ROW(i) P3_MM(i, 0); P3_MM(i, 1); P3_MM(i, 2); P3_MM(i, 3); P3_MM(i, 4)
    P3_LD(1); SBAR(); __builtin_amdgcn_s_setprio(3); P3_ROW(0); SBAR();
    P3_LD(2); SBAR(); __builtin_amdgcn_s_setprio(2); P3_ROW(1); SBAR();
    P3_LD(3); SBAR(); __builtin_amdgcn_s_setprio(1); P3_ROW(2); __builtin_amdgcn_s_setprio(0); P3_ROW(3);
    __syncthreads();
    auto mix = [&](int r, const f32x4 (&p)[8]) {
        const f32x4 a = (p[0] + p[1]) + (p[2] + p[3]), c = (p[4] + p[5]) + (p[6] + p[7]);
        const f32x4 o = rnd4(rnd4(a) * gv[r]) + rnd4(rnd4(c) * g1[r]);
        __builtin_amdgcn_raw_buffer_store_b64(u32x2{cvtpk(o[0], o[1]), cvtpk(o[2], o[3])}, outr, oo[r], 0, 0);
    };
    f32x4 p0[8], p1[8], p2[8];
#pragma unroll
    for (int k = 0; k < 8; ++k) p0[k] = *(const f32x4*)(lds + (k * 20 + w) * 1024 + lane * 16);
#pragma unroll
    for (int k = 0; k < 8; ++k) p1[k] = *(const f32x4*)(lds + (k * 20 + 8 + w) * 1024 + lane * 16);
    if (w < 4) {
#pragma unroll
        for (int k = 0; k < 8; ++k) p2[k] = *(const f32x4*)(lds + (k * 20 + 16 + w) * 1024 + lane * 16);
    }
    SBAR();
    mix(0, p0); mix(1, p1);
    if (w < 4) mix(2, p2);
    asm volatile("gm_text_end:");
}

extern "C" void kernel_launch(void* const* d_in, const int* in_sizes, int n_in, void* d_out, int out_size, void* d_ws, size_t ws_size, hipStream_t stream) {
    const u16* x = (const u16*)d_in[0];
    const u16* wqA = (const u16*)d_in[1];
    const u16* wkA = (const u16*)d_in[2];
    const u16* wvA = (const u16*)d_in[3];
    const u16* woA = (const u16*)d_in[4];
    const u16* wqkv = (const u16*)d_in[6];
    const u16* woB = (const u16*)d_in[7];
    const u16* gw = (const u16*)d_in[8];
    char* ws = (char*)d_ws;
    gm_k1_proj<<<256, 512, 0, stream>>>(x, wqA, wkA, wvA, wqkv, gw, woA, woB, ws);
    gm_k2_attn<<<256, 512, 0, stream>>>(ws, (u16*)d_out);
}
```
